```python
import torch

def _softmax(x, axis=-1):
    return torch.softmax(x, dim=axis)
def _gelu(x):
    return torch.nn.functional.gelu(x, approximate="tanh")
def _silu(x):
    return torch.nn.functional.silu(x)
def _sigmoid(x):
    return torch.sigmoid(x)
def _layernorm(x, axis=-1, eps=1e-5):
    m = x.mean(dim=axis, keepdim=True)
    v = x.var(dim=axis, keepdim=True, unbiased=False)
    return (x - m) / torch.sqrt(v + eps)
def _scatter_add(tgt, idx, src, axis=0):
    return tgt.clone().index_add_(axis, idx, src)
def _segment_sum(src, seg, n):
    out = torch.zeros((n,) + src.shape[1:], dtype=src.dtype)
    return out.index_add_(0, seg, src)
def _pad(x, pw):
    flat = tuple(v for pair in reversed(pw) for v in pair)
    return torch.nn.functional.pad(x, flat)

kernel_name = 'parallel_sum__mha_block_cached__glu_gated_bilinear__decode'

def setup_inputs():

    _w = (lambda a: (lambda t: t.to(torch.bfloat16) if t.is_floating_point() else t)(torch.as_tensor(a)))
    def _A_setup():
        B, Sq, D = 8, 1, 1024
        H, Sk, Dh = 32, 4096, 128
        return {
            "x": _w((lambda *s: torch.randn(*s, dtype=torch.bfloat16) * 0.05)(B, Sq, D)),
            "k_cache": _w((lambda *s: torch.randn(*s, dtype=torch.bfloat16) * 1.6)(B, H, Sk, Dh)),
            "v_cache": _w((lambda *s: torch.randn(*s, dtype=torch.bfloat16) * 0.8)(B, H, Sk, Dh)),
            "wq": _w((lambda *s: torch.randn(*s, dtype=torch.bfloat16) * 1.6)(D, H * Dh) * (D ** -0.5)),
            "wo": _w((lambda *s: torch.randn(*s, dtype=torch.bfloat16) * 0.8)(H * Dh, D) * ((H * Dh) ** -0.5)),
        }
    _A_in = _A_setup()

    def _B_setup():
        B, S, D, F = 8, 1, 1024, 3584
        return {
            "x": _w((lambda *s: torch.randn(*s, dtype=torch.bfloat16) * 0.05)(B, S, D)),
            "w_gate": _w((lambda *s: torch.randn(*s, dtype=torch.bfloat16) * 0.4)(D, F) * (D ** -0.5)),
            "w_up": _w((lambda *s: torch.randn(*s, dtype=torch.bfloat16) * 0.4)(D, F) * (D ** -0.5)),
            "w_down": _w((lambda *s: torch.randn(*s, dtype=torch.bfloat16) * 0.4)(F, D) * (F ** -0.5)),
        }
    _B_in = _B_setup()

    return {
        "A_x": _A_in["x"],
        "A_k_cache": _A_in["k_cache"],
        "A_v_cache": _A_in["v_cache"],
        "A_wq": _A_in["wq"],
        "A_wo": _A_in["wo"],
        "B_x": _B_in["x"],
        "B_w_gate": _B_in["w_gate"],
        "B_w_up": _B_in["w_up"],
        "B_w_down": _B_in["w_down"],
    }

def reference(A_x, A_k_cache, A_v_cache, A_wq, A_wo, B_x, B_w_gate, B_w_up, B_w_down):
    def _A_ref(x, k_cache, v_cache, wq, wo):
        B, Sq, D = x.shape
        H, Dh = 32, 128
        q = (lambda x, *d: x.permute(*d))((x @ wq).reshape(B, Sq, H, Dh), 0, 2, 1, 3)
        scores = torch.einsum("bhqd,bhkd->bhqk", q, k_cache) * (Dh ** -0.5)
        p = _softmax(scores, axis=-1)
        o = torch.einsum("bhqk,bhkd->bhqd", p, v_cache)
        return (lambda x, *d: x.permute(*d))(o, 0, 2, 1, 3).reshape(B, Sq, H * Dh) @ wo

    def _B_ref(x, w_gate, w_up, w_down):
        g = torch.einsum("bsd,df->bsf", x, w_gate)
        u = torch.einsum("bsd,df->bsf", x, w_up)
        h = g * u
        return torch.einsum("bsf,fd->bsd", h, w_down)

    x = A_x
    xn = _layernorm(x, axis=-1)
    return x + _A_ref(xn, A_k_cache, A_v_cache, A_wq, A_wo) + _B_ref(xn, B_w_gate, B_w_up, B_w_down)
```

```cpp
#include <hip/hip_runtime.h>
#include <hip/hip_bf16.h>
#include <stdint.h>

#define MIMIC 0

typedef unsigned u32x4 __attribute__((ext_vector_type(4)));
typedef __bf16 bf16x2 __attribute__((ext_vector_type(2)));
typedef float f32x2 __attribute__((ext_vector_type(2)));

constexpr int B = 8, D = 1024, H = 32, SK = 4096, DH = 128, F = 3584;

__device__ __forceinline__ float bf_lo(unsigned u) { return __uint_as_float(u << 16); }
__device__ __forceinline__ float bf_hi(unsigned u) { return __uint_as_float(u & 0xffff0000u); }
__device__ __forceinline__ float rbf(float x) {
  __bf16 b = (__bf16)x;
  return (float)b;
}
__device__ __forceinline__ unsigned short f2bf(float x) {
  __bf16 b = (__bf16)x;
  return __builtin_bit_cast(unsigned short, b);
}
__device__ __forceinline__ float dot2(unsigned k, unsigned q, float acc) {
  return __builtin_amdgcn_fdot2_f32_bf16(__builtin_bit_cast(bf16x2, k), __builtin_bit_cast(bf16x2, q), acc, false);
}
template <int CTRL> __device__ __forceinline__ float dpp(float x) {
  return __builtin_bit_cast(float, __builtin_amdgcn_mov_dpp(__builtin_bit_cast(int, x), CTRL, 0xf, 0xf, true));
}
constexpr int XOR1 = 0xB1, XOR2 = 0x4E, XOR7 = 0x141, XOR8 = 0x128;

__global__ void __launch_bounds__(256) k1(const unsigned short* __restrict__ x, const unsigned short* __restrict__ wq,
                                          const unsigned short* __restrict__ wg, const unsigned short* __restrict__ wu,
                                          unsigned short* __restrict__ qout, unsigned short* __restrict__ hout) {
  __shared__ float xn[B][D];
  __shared__ float red[32][B][64 + 1];
  const int tid = threadIdx.x, lane = tid & 63, wave = tid >> 6;
  for (int bb = 0; bb < 2; ++bb) {
    const int b = wave * 2 + bb;
    float v[16];
    const u32x4* p = (const u32x4*)(x + b * D);
    u32x4 a0 = p[lane], a1 = p[64 + lane];
    for (int k = 0; k < 4; ++k) { v[2 * k] = bf_lo(a0[k]); v[2 * k + 1] = bf_hi(a0[k]); v[8 + 2 * k] = bf_lo(a1[k]); v[8 + 2 * k + 1] = bf_hi(a1[k]); }
    float s = 0;
    for (int k = 0; k < 16; ++k) s += v[k];
    for (int o = 32; o; o >>= 1) s += __shfl_xor(s, o);
    const float mean = s * (1.f / D);
    float s2 = 0;
    for (int k = 0; k < 16; ++k) s2 += (v[k] - mean) * (v[k] - mean);
    for (int o = 32; o; o >>= 1) s2 += __shfl_xor(s2, o);
    const float var = s2 * (1.f / D);
    if (MIMIC) {
      const float m = rbf(mean), den = rbf(sqrtf(rbf(rbf(var) + 1e-5f)));
      for (int k = 0; k < 8; ++k) { xn[b][lane * 8 + k] = rbf(rbf(v[k] - m) / den); xn[b][512 + lane * 8 + k] = rbf(rbf(v[8 + k] - m) / den); }
    } else {
      const float r = 1.f / sqrtf(var + 1e-5f);
      for (int k = 0; k < 8; ++k) { xn[b][lane * 8 + k] = (v[k] - mean) * r; xn[b][512 + lane * 8 + k] = (v[8 + k] - mean) * r; }
    }
  }
  __syncthreads();
  const int blk = blockIdx.x;
  const bool isq = blk < 64;
  const int c8 = tid & 7, r = tid >> 3;
  const unsigned short* w; int ld;
  if (isq) { w = wq + blk * 64 + c8 * 8; ld = H * DH; }
  else { const int j = blk - 64; w = (c8 < 4 ? wg : wu) + j * 32 + (c8 & 3) * 8; ld = F; }
  float acc[B][8];
  for (int b = 0; b < B; ++b) for (int k = 0; k < 8; ++k) acc[b][k] = 0;
#pragma unroll 8
  for (int i = 0; i < 32; ++i) {
    const int row = r + 32 * i;
    u32x4 wv = *(const u32x4*)(w + (size_t)row * ld);
    float wf[8];
    for (int k = 0; k < 4; ++k) { wf[2 * k] = bf_lo(wv[k]); wf[2 * k + 1] = bf_hi(wv[k]); }
    for (int b = 0; b < B; ++b) { const float xv = xn[b][row]; for (int k = 0; k < 8; ++k) acc[b][k] = fmaf(xv, wf[k], acc[b][k]); }
  }
  for (int b = 0; b < B; ++b) for (int k = 0; k < 8; ++k) red[r][b][c8 * 8 + k] = acc[b][k];
  __syncthreads();
  if (isq) {
    for (int o = tid; o < B * 64; o += 256) {
      const int b = o >> 6, c = o & 63;
      float s = 0; for (int rr = 0; rr < 32; ++rr) s += red[rr][b][c];
      qout[b * (H * DH) + blk * 64 + c] = f2bf(s);
    }
  } else {
    const int j = blk - 64;
    const int b = tid >> 5, c = tid & 31;
    float g = 0, u = 0;
    for (int rr = 0; rr < 32; ++rr) { g += red[rr][b][c]; u += red[rr][b][32 + c]; }
    if (MIMIC) { g = rbf(g); u = rbf(u); }
    hout[b * F + j * 32 + c] = f2bf(g * u);
  }
}

constexpr int NW = 8, T = 8;
__global__ void __launch_bounds__(NW * 64) k2(const unsigned short* __restrict__ q, const unsigned short* __restrict__ kc,
                                              const unsigned short* __restrict__ vc, unsigned short* __restrict__ o) {
  __shared__ float mrg[NW * 4][DH + 2];
  const int tid = threadIdx.x, lane = tid & 63, wave = __builtin_amdgcn_readfirstlane(tid >> 6), r = lane >> 4, c = lane & 15;
  const int bh = blockIdx.x;
  const u32x4 qv = *(const u32x4*)(q + (size_t)bh * DH + 8 * c);
  const u32x4* kp = (const u32x4*)(kc + (size_t)bh * SK * DH) + lane;
  const u32x4* vp = (const u32x4*)(vc + (size_t)bh * SK * DH) + lane;
  constexpr int NT = SK / (4 * T) / NW;
  constexpr float C2 = 0.08838834764831845f * 1.4426950408889634f;
  u32x4 K[T], V[T];
  {
    const int base = wave * T;
#pragma unroll
    for (int i = 0; i < T; ++i) K[i] = __builtin_nontemporal_load(kp + (size_t)(base + i) * 64);
#pragma unroll
    for (int i = 0; i < T; ++i) V[i] = __builtin_nontemporal_load(vp + (size_t)(base + i) * 64);
  }
  float m = -INFINITY, l = 0.f, acc[8];
#pragma unroll
  for (int k = 0; k < 8; ++k) acc[k] = 0.f;
  for (int t = 0; t < NT; ++t) {
    const int nb = ((t + 1 < NT ? t + 1 : t) * NW + wave) * T;
    float s[T];
#pragma unroll
    for (int i = 0; i < T; ++i) {
      float d = 0.f;
#pragma unroll
      for (int k = 0; k < 4; ++k) d = dot2(K[i][k], qv[k], d);
      s[i] = d;
    }
    if (t + 1 < NT) {
#pragma unroll
      for (int i = 0; i < T; ++i) K[i] = __builtin_nontemporal_load(kp + (size_t)(nb + i) * 64);
    }
    float mx = m;
#pragma unroll
    for (int i = 0; i < T; ++i) {
      float d = s[i];
      d += dpp<XOR1>(d); d += dpp<XOR2>(d); d += dpp<XOR7>(d); d += dpp<XOR8>(d);
      if (MIMIC) { d = rbf(rbf(d) * 0.08838834764831845f) * 1.4426950408889634f; } else d *= C2;
      s[i] = d; mx = fmaxf(mx, d);
    }
    const float alpha = __builtin_amdgcn_exp2f(m - mx);
    m = mx; l *= alpha;
#pragma unroll
    for (int k = 0; k < 8; ++k) acc[k] *= alpha;
#pragma unroll
    for (int i = 0; i < T; ++i) {
      const float p = __builtin_amdgcn_exp2f(s[i] - mx);
      l += p;
#pragma unroll
      for (int k = 0; k < 4; ++k) { acc[2 * k] = fmaf(p, bf_lo(V[i][k]), acc[2 * k]); acc[2 * k + 1] = fmaf(p, bf_hi(V[i][k]), acc[2 * k + 1]); }
    }
    if (t + 1 < NT) {
#pragma unroll
      for (int i = 0; i < T; ++i) V[i] = __builtin_nontemporal_load(vp + (size_t)(nb + i) * 64);
    }
  }
  float* rec = mrg[wave * 4 + r];
#pragma unroll
  for (int k = 0; k < 8; ++k) rec[8 * c + k] = acc[k];
  if (c == 0) { rec[DH] = m; rec[DH + 1] = l; }
  __syncthreads();
  if (tid < DH) {
    float M = -INFINITY;
    for (int j = 0; j < NW * 4; ++j) M = fmaxf(M, mrg[j][DH]);
    float L = 0, a = 0;
    for (int j = 0; j < NW * 4; ++j) { const float w = __builtin_amdgcn_exp2f(mrg[j][DH] - M); L = fmaf(w, mrg[j][DH + 1], L); a = fmaf(w, mrg[j][tid], a); }
    o[(size_t)bh * DH + tid] = f2bf(a / L);
  }
}

__global__ void __launch_bounds__(256) k3(const unsigned short* __restrict__ x, const unsigned short* __restrict__ o, const unsigned short* __restrict__ h,
                                          const unsigned short* __restrict__ wo, const unsigned short* __restrict__ wd, unsigned short* __restrict__ out) {
  __shared__ float red[2][256][B * 8 + 1];
  const int tid = threadIdx.x;
  const int c0 = blockIdx.x * 8;
  float a1[B][8], a2[B][8];
  for (int b = 0; b < B; ++b) for (int k = 0; k < 8; ++k) { a1[b][k] = 0; a2[b][k] = 0; }
#pragma unroll 4
  for (int row = tid; row < H * DH; row += 256) {
    u32x4 wv = *(const u32x4*)(wo + (size_t)row * D + c0);
    float wf[8];
    for (int k = 0; k < 4; ++k) { wf[2 * k] = bf_lo(wv[k]); wf[2 * k + 1] = bf_hi(wv[k]); }
    for (int b = 0; b < B; ++b) { const float xv = __uint_as_float((unsigned)o[b * (H * DH) + row] << 16); for (int k = 0; k < 8; ++k) a1[b][k] = fmaf(xv, wf[k], a1[b][k]); }
  }
#pragma unroll 2
  for (int row = tid; row < F; row += 256) {
    u32x4 wv = *(const u32x4*)(wd + (size_t)row * D + c0);
    float wf[8];
    for (int k = 0; k < 4; ++k) { wf[2 * k] = bf_lo(wv[k]); wf[2 * k + 1] = bf_hi(wv[k]); }
    for (int b = 0; b < B; ++b) { const float xv = __uint_as_float((unsigned)h[b * F + row] << 16); for (int k = 0; k < 8; ++k) a2[b][k] = fmaf(xv, wf[k], a2[b][k]); }
  }
  for (int b = 0; b < B; ++b) for (int k = 0; k < 8; ++k) { red[0][tid][b * 8 + k] = a1[b][k]; red[1][tid][b * 8 + k] = a2[b][k]; }
  __syncthreads();
  if (tid < 64) {
    float s1 = 0, s2 = 0;
    for (int j = 0; j < 256; ++j) { s1 += red[0][j][tid]; s2 += red[1][j][tid]; }
    const int b = tid >> 3, k = tid & 7;
    const float xv = __uint_as_float((unsigned)x[b * D + c0 + k] << 16);
    float r;
    if (MIMIC) r = rbf(xv + rbf(s1)) + rbf(s2); else r = xv + s1 + s2;
    out[b * D + c0 + k] = f2bf(r);
  }
}

extern "C" void kernel_launch(void* const* d_in, const int* in_sizes, int n_in, void* d_out, int out_size, void* d_ws, size_t ws_size,
                              hipStream_t stream) {
  const unsigned short* x = (const unsigned short*)d_in[0];
  const unsigned short* kc = (const unsigned short*)d_in[1];
  const unsigned short* vc = (const unsigned short*)d_in[2];
  const unsigned short* wq = (const unsigned short*)d_in[3];
  const unsigned short* wo = (const unsigned short*)d_in[4];
  const unsigned short* wg = (const unsigned short*)d_in[6];
  const unsigned short* wu = (const unsigned short*)d_in[7];
  const unsigned short* wd = (const unsigned short*)d_in[8];
  unsigned short* q = (unsigned short*)d_ws;
  unsigned short* h = q + B * H * DH;
  unsigned short* o = h + B * F;
  k1<<<64 + F / 32, 256, 0, stream>>>(x, wq, wg, wu, q, h);
  k2<<<B * H, NW * 64, 0, stream>>>(q, kc, vc, o);
  k3<<<D / 8, 256, 0, stream>>>(x, o, h, wo, wd, (unsigned short*)d_out);
}
```

```cpp
#include <hip/hip_runtime.h>
#include <hip/hip_bf16.h>
#include <stdint.h>

#define MIMIC 1

typedef unsigned u32x4 __attribute__((ext_vector_type(4)));
typedef __bf16 bf16x2 __attribute__((ext_vector_type(2)));
typedef float f32x2 __attribute__((ext_vector_type(2)));

constexpr int B = 8, D = 1024, H = 32, SK = 4096, DH = 128, F = 3584;

__device__ __forceinline__ float bf_lo(unsigned u) { return __uint_as_float(u << 16); }
__device__ __forceinline__ float bf_hi(unsigned u) { return __uint_as_float(u & 0xffff0000u); }
__device__ __forceinline__ float rbf(float x) {
  __bf16 b = (__bf16)x;
  return (float)b;
}
__device__ __forceinline__ unsigned short f2bf(float x) {
  __bf16 b = (__bf16)x;
  return __builtin_bit_cast(unsigned short, b);
}
__device__ __forceinline__ float dot2(unsigned k, unsigned q, float acc) {
  return __builtin_amdgcn_fdot2_f32_bf16(__builtin_bit_cast(bf16x2, k), __builtin_bit_cast(bf16x2, q), acc, false);
}
template <int CTRL> __device__ __forceinline__ float dpp(float x) {
  return __builtin_bit_cast(float, __builtin_amdgcn_mov_dpp(__builtin_bit_cast(int, x), CTRL, 0xf, 0xf, true));
}
constexpr int XOR1 = 0xB1, XOR2 = 0x4E, XOR7 = 0x141, XOR8 = 0x128;

__global__ void __launch_bounds__(256) k1(const unsigned short* __restrict__ x, const unsigned short* __restrict__ wq,
                                          const unsigned short* __restrict__ wg, const unsigned short* __restrict__ wu,
                                          unsigned short* __restrict__ qout, unsigned short* __restrict__ hout) {
  __shared__ float xn[B][D];
  __shared__ float red[32][B][64 + 1];
  const int tid = threadIdx.x, lane = tid & 63, wave = tid >> 6;
  for (int bb = 0; bb < 2; ++bb) {
    const int b = wave * 2 + bb;
    float v[16];
    const u32x4* p = (const u32x4*)(x + b * D);
    u32x4 a0 = p[lane], a1 = p[64 + lane];
    for (int k = 0; k < 4; ++k) { v[2 * k] = bf_lo(a0[k]); v[2 * k + 1] = bf_hi(a0[k]); v[8 + 2 * k] = bf_lo(a1[k]); v[8 + 2 * k + 1] = bf_hi(a1[k]); }
    float s = 0;
    for (int k = 0; k < 16; ++k) s += v[k];
    for (int o = 32; o; o >>= 1) s += __shfl_xor(s, o);
    const float mean = s * (1.f / D);
    float s2 = 0;
    for (int k = 0; k < 16; ++k) s2 += (v[k] - mean) * (v[k] - mean);
    for (int o = 32; o; o >>= 1) s2 += __shfl_xor(s2, o);
    const float var = s2 * (1.f / D);
    if (MIMIC) {
      const float m = rbf(mean), den = rbf(sqrtf(rbf(rbf(var) + 1e-5f)));
      for (int k = 0; k < 8; ++k) { xn[b][lane * 8 + k] = rbf(rbf(v[k] - m) / den); xn[b][512 + lane * 8 + k] = rbf(rbf(v[8 + k] - m) / den); }
    } else {
      const float r = 1.f / sqrtf(var + 1e-5f);
      for (int k = 0; k < 8; ++k) { xn[b][lane * 8 + k] = (v[k] - mean) * r; xn[b][512 + lane * 8 + k] = (v[8 + k] - mean) * r; }
    }
  }
  __syncthreads();
  const int blk = blockIdx.x;
  const bool isq = blk < 64;
  const int c8 = tid & 7, r = tid >> 3;
  const unsigned short* w; int ld;
  if (isq) { w = wq + blk * 64 + c8 * 8; ld = H * DH; }
  else { const int j = blk - 64; w = (c8 < 4 ? wg : wu) + j * 32 + (c8 & 3) * 8; ld = F; }
  float acc[B][8];
  for (int b = 0; b < B; ++b) for (int k = 0; k < 8; ++k) acc[b][k] = 0;
#pragma unroll 8
  for (int i = 0; i < 32; ++i) {
    const int row = r + 32 * i;
    u32x4 wv = __builtin_nontemporal_load((const u32x4*)(w + (size_t)row * ld));
    float wf[8];
    for (int k = 0; k < 4; ++k) { wf[2 * k] = bf_lo(wv[k]); wf[2 * k + 1] = bf_hi(wv[k]); }
    for (int b = 0; b < B; ++b) { const float xv = xn[b][row]; for (int k = 0; k < 8; ++k) acc[b][k] = fmaf(xv, wf[k], acc[b][k]); }
  }
  for (int b = 0; b < B; ++b) for (int k = 0; k < 8; ++k) red[r][b][c8 * 8 + k] = acc[b][k];
  __syncthreads();
  if (isq) {
    for (int o = tid; o < B * 64; o += 256) {
      const int b = o >> 6, c = o & 63;
      float s = 0; for (int rr = 0; rr < 32; ++rr) s += red[rr][b][c];
      qout[b * (H * DH) + blk * 64 + c] = f2bf(s);
    }
  } else {
    const int j = blk - 64;
    const int b = tid >> 5, c = tid & 31;
    float g = 0, u = 0;
    for (int rr = 0; rr < 32; ++rr) { g += red[rr][b][c]; u += red[rr][b][32 + c]; }
    if (MIMIC) { g = rbf(g); u = rbf(u); }
    hout[b * F + j * 32 + c] = f2bf(g * u);
  }
}

constexpr int NW = 4, T = 4;
__global__ void __launch_bounds__(NW * 64) k2(const unsigned short* __restrict__ q, const unsigned short* __restrict__ kc,
                                              const unsigned short* __restrict__ vc, unsigned short* __restrict__ o) {
  __shared__ float mrg[NW * 4][DH + 2];
  const int tid = threadIdx.x, lane = tid & 63, wave = __builtin_amdgcn_readfirstlane(tid >> 6), r = lane >> 4, c = lane & 15;
  const int bh = blockIdx.x;
  const u32x4 qv = *(const u32x4*)(q + (size_t)bh * DH + 8 * c);
  const u32x4* kp = (const u32x4*)(kc + (size_t)bh * SK * DH) + lane;
  const u32x4* vp = (const u32x4*)(vc + (size_t)bh * SK * DH) + lane;
  constexpr int NT = SK / (4 * T) / NW;
  constexpr float C2 = 0.08838834764831845f * 1.4426950408889634f;
  u32x4 K[T], V[T];
  {
    const int base = wave * T;
#pragma unroll
    for (int i = 0; i < T; ++i) K[i] = __builtin_nontemporal_load(kp + (size_t)(base + i) * 64);
#pragma unroll
    for (int i = 0; i < T; ++i) V[i] = __builtin_nontemporal_load(vp + (size_t)(base + i) * 64);
  }
  float m = -INFINITY, l = 0.f, acc[8];
#pragma unroll
  for (int k = 0; k < 8; ++k) acc[k] = 0.f;
  for (int t = 0; t < NT; ++t) {
    const int nb = ((t + 1 < NT ? t + 1 : t) * NW + wave) * T;
    float s[T];
#pragma unroll
    for (int i = 0; i < T; ++i) {
      float d = 0.f;
#pragma unroll
      for (int k = 0; k < 4; ++k) d = dot2(K[i][k], qv[k], d);
      s[i] = d;
    }
    if (t + 1 < NT) {
#pragma unroll
      for (int i = 0; i < T; ++i) K[i] = __builtin_nontemporal_load(kp + (size_t)(nb + i) * 64);
    }
    float mx = m;
#pragma unroll
    for (int i = 0; i < T; ++i) {
      float d = s[i];
      d += dpp<XOR1>(d); d += dpp<XOR2>(d); d += dpp<XOR7>(d); d += dpp<XOR8>(d);
      if (MIMIC) { d = rbf(rbf(d) * 0.08838834764831845f) * 1.4426950408889634f; } else d *= C2;
      s[i] = d; mx = fmaxf(mx, d);
    }
    const float alpha = __builtin_amdgcn_exp2f(m - mx);
    m = mx; l *= alpha;
#pragma unroll
    for (int k = 0; k < 8; ++k) acc[k] *= alpha;
#pragma unroll
    for (int i = 0; i < T; ++i) {
      const float p = __builtin_amdgcn_exp2f(s[i] - mx);
      l += p;
#pragma unroll
      for (int k = 0; k < 4; ++k) { acc[2 * k] = fmaf(p, bf_lo(V[i][k]), acc[2 * k]); acc[2 * k + 1] = fmaf(p, bf_hi(V[i][k]), acc[2 * k + 1]); }
    }
    if (t + 1 < NT) {
#pragma unroll
      for (int i = 0; i < T; ++i) V[i] = __builtin_nontemporal_load(vp + (size_t)(nb + i) * 64);
    }
  }
  float* rec = mrg[wave * 4 + r];
#pragma unroll
  for (int k = 0; k < 8; ++k) rec[8 * c + k] = acc[k];
  if (c == 0) { rec[DH] = m; rec[DH + 1] = l; }
  __syncthreads();
  if (tid < DH) {
    float M = -INFINITY;
    for (int j = 0; j < NW * 4; ++j) M = fmaxf(M, mrg[j][DH]);
    float L = 0, a = 0;
    for (int j = 0; j < NW * 4; ++j) { const float w = __builtin_amdgcn_exp2f(mrg[j][DH] - M); L = fmaf(w, mrg[j][DH + 1], L); a = fmaf(w, mrg[j][tid], a); }
    o[(size_t)bh * DH + tid] = f2bf(a / L);
  }
}

__global__ void __launch_bounds__(256) k3(const unsigned short* __restrict__ x, const unsigned short* __restrict__ o, const unsigned short* __restrict__ h,
                                          const unsigned short* __restrict__ wo, const unsigned short* __restrict__ wd, unsigned short* __restrict__ out) {
  __shared__ float red[2][256][B * 8 + 1];
  const int tid = threadIdx.x;
  const int c0 = blockIdx.x * 8;
  float a1[B][8], a2[B][8];
  for (int b = 0; b < B; ++b) for (int k = 0; k < 8; ++k) { a1[b][k] = 0; a2[b][k] = 0; }
#pragma unroll 4
  for (int row = tid; row < H * DH; row += 256) {
    u32x4 wv = __builtin_nontemporal_load((const u32x4*)(wo + (size_t)row * D + c0));
    float wf[8];
    for (int k = 0; k < 4; ++k) { wf[2 * k] = bf_lo(wv[k]); wf[2 * k + 1] = bf_hi(wv[k]); }
    for (int b = 0; b < B; ++b) { const float xv = __uint_as_float((unsigned)o[b * (H * DH) + row] << 16); for (int k = 0; k < 8; ++k) a1[b][k] = fmaf(xv, wf[k], a1[b][k]); }
  }
#pragma unroll 2
  for (int row = tid; row < F; row += 256) {
    u32x4 wv = __builtin_nontemporal_load((const u32x4*)(wd + (size_t)row * D + c0));
    float wf[8];
    for (int k = 0; k < 4; ++k) { wf[2 * k] = bf_lo(wv[k]); wf[2 * k + 1] = bf_hi(wv[k]); }
    for (int b = 0; b < B; ++b) { const float xv = __uint_as_float((unsigned)h[b * F + row] << 16); for (int k = 0; k < 8; ++k) a2[b][k] = fmaf(xv, wf[k], a2[b][k]); }
  }
  for (int b = 0; b < B; ++b) for (int k = 0; k < 8; ++k) { red[0][tid][b * 8 + k] = a1[b][k]; red[1][tid][b * 8 + k] = a2[b][k]; }
  __syncthreads();
  if (tid < 64) {
    float s1 = 0, s2 = 0;
    for (int j = 0; j < 256; ++j) { s1 += red[0][j][tid]; s2 += red[1][j][tid]; }
    const int b = tid >> 3, k = tid & 7;
    const float xv = __uint_as_float((unsigned)x[b * D + c0 + k] << 16);
    float r;
    if (MIMIC) r = rbf(xv + rbf(s1)) + rbf(s2); else r = xv + s1 + s2;
    out[b * D + c0 + k] = f2bf(r);
  }
}

extern "C" void kernel_launch(void* const* d_in, const int* in_sizes, int n_in, void* d_out, int out_size, void* d_ws, size_t ws_size,
                              hipStream_t stream) {
  const unsigned short* x = (const unsigned short*)d_in[0];
  const unsigned short* kc = (const unsigned short*)d_in[1];
  const unsigned short* vc = (const unsigned short*)d_in[2];
  const unsigned short* wq = (const unsigned short*)d_in[3];
  const unsigned short* wo = (const unsigned short*)d_in[4];
  const unsigned short* wg = (const unsigned short*)d_in[6];
  const unsigned short* wu = (const unsigned short*)d_in[7];
  const unsigned short* wd = (const unsigned short*)d_in[8];
  unsigned short* q = (unsigned short*)d_ws;
  unsigned short* h = q + B * H * DH;
  unsigned short* o = h + B * F;
  k1<<<64 + F / 32, 256, 0, stream>>>(x, wq, wg, wu, q, h);
  k2<<<B * H, NW * 64, 0, stream>>>(q, kc, vc, o);
  k3<<<D / 8, 256, 0, stream>>>(x, o, h, wo, wd, (unsigned short*)d_out);
}
```

```cpp
#include <hip/hip_runtime.h>
#include <hip/hip_bf16.h>
#include <stdint.h>

#define STAMPS 0
#define KV_T 4
#define POLL_SLEEP 2
#define WIN_Q 8
#define WIN_F 8
#define WIN_S 6
#define PRE 0
#define NX 0
#define WIN_K 8
#define WIN_KV 5
#define ABL 0
#define TROT 0
#define KV_T0 420
#define WMASK 0
#define WIN_KK(i) (WIN_KV + ((WMASK >> (i)) & 1))
#define WIN_VV(i) (WIN_KV + ((WMASK >> (4 + (i))) & 1))
#define CODE_PF 0

namespace {

typedef unsigned u32x4 __attribute__((ext_vector_type(4)));
typedef unsigned u32x2 __attribute__((ext_vector_type(2)));
typedef float f32x4 __attribute__((ext_vector_type(4)));
typedef float f32x2 __attribute__((ext_vector_type(2)));
typedef __bf16 bf16x2 __attribute__((ext_vector_type(2)));
typedef __bf16 bf16x8 __attribute__((ext_vector_type(8)));
typedef unsigned long long u64;
#define GAS __attribute__((address_space(1)))
#define LAS __attribute__((address_space(3)))
#define RLX_WG __ATOMIC_RELAXED, __HIP_MEMORY_SCOPE_WORKGROUP
typedef __amdgpu_buffer_rsrc_t rsrc_t;

constexpr int B = 8, D = 1024, H = 32, SK = 4096, DH = 128, F = 3584;
constexpr int FS = F / 32;
constexpr int NQT = H * DH / 64, NFT = F / 64;
constexpr int AUX_NT = 2, AUX_SC1 = 16;
constexpr int NS = 4, T = KV_T;
constexpr int NT = (SK / 4) / (NS * T);

constexpr int WS_EPOCH = 0;
constexpr int WS_Q = 4096;
constexpr int WS_G = WS_Q + B * H * 2 * DH * 8;
constexpr int WS_U = WS_G + B * F * 4;
constexpr int WS_O = WS_U + B * F * 4;
constexpr int WS_P = WS_O + B * H * DH * 4;
constexpr int WS_END = WS_P + 256 * 8192;
constexpr int WS_STAMP = 16 << 20;

constexpr u64 SPIN_LIMIT = 2000000;

constexpr int XN_STRIDE = 516;
constexpr int L_XN = 0;
constexpr int L_P1 = L_XN + 8 * XN_STRIDE;
constexpr int L_A6 = L_P1 + 2048;
constexpr int L_ACC = L_A6 + 2048;
constexpr int L_WO = L_ACC + 1024;
constexpr int AI_STRIDE = 68;
constexpr int L_AI = L_WO + 4096;
constexpr int L_Q = L_AI + 16 * AI_STRIDE;
constexpr int L_REC = L_Q + 64;
constexpr int REC_STRIDE = 132;
constexpr int L_FX = L_REC + NS * REC_STRIDE;
constexpr int L_FP = L_FX + 1, L_FQ = L_FX + 2, L_FS = L_FX + 3, L_FA = L_FX + 4, L_FW = L_FX + 5, L_FO = L_FX + 6, L_F6 = L_FX + 7;
constexpr int L_END = L_FX + 8;

__device__ __forceinline__ float bf_lo(unsigned u) { return __uint_as_float(u << 16); }
__device__ __forceinline__ float bf_hi(unsigned u) { return __uint_as_float(u & 0xffff0000u); }
__device__ __forceinline__ unsigned pk_bf16(float lo, float hi) {
  const f32x2 v = {lo, hi};
  return __builtin_bit_cast(unsigned, __builtin_convertvector(v, bf16x2));
}
__device__ __forceinline__ float rbf(float x) { return bf_lo(pk_bf16(x, x)); }
__device__ __forceinline__ float dot2(unsigned k, unsigned q, float acc) {
  return __builtin_amdgcn_fdot2_f32_bf16(__builtin_bit_cast(bf16x2, k), __builtin_bit_cast(bf16x2, q), acc, false);
}
template <int CTRL> __device__ __forceinline__ unsigned dppu(unsigned x) { return (unsigned)__builtin_amdgcn_mov_dpp((int)x, CTRL, 0xf, 0xf, true); }
template <int CTRL> __device__ __forceinline__ float dpp(float x) { return __uint_as_float(dppu<CTRL>(__float_as_uint(x))); }
constexpr int XOR1 = 0xB1, XOR2 = 0x4E, XOR7 = 0x141, XOR8 = 0x128;
__device__ __forceinline__ float row16_sum(float d) {
  d += dpp<XOR1>(d); d += dpp<XOR2>(d); d += dpp<XOR7>(d); d += dpp<XOR8>(d);
  return d;
}
__device__ __forceinline__ float xrow_sum(float x) {
  auto s = __builtin_amdgcn_permlane16_swap(__float_as_uint(x), __float_as_uint(x), false, false);
  x = __uint_as_float(s[0]) + __uint_as_float(s[1]);
  auto t = __builtin_amdgcn_permlane32_swap(__float_as_uint(x), __float_as_uint(x), false, false);
  return __uint_as_float(t[0]) + __uint_as_float(t[1]);
}
__device__ __forceinline__ float xrow_max(float x) {
  auto s = __builtin_amdgcn_permlane16_swap(__float_as_uint(x), __float_as_uint(x), false, false);
  x = fmaxf(__uint_as_float(s[0]), __uint_as_float(s[1]));
  auto t = __builtin_amdgcn_permlane32_swap(__float_as_uint(x), __float_as_uint(x), false, false);
  return fmaxf(__uint_as_float(t[0]), __uint_as_float(t[1]));
}
__device__ __forceinline__ rsrc_t mk_rsrc(const void* p, unsigned bytes) {
  return __builtin_amdgcn_make_buffer_rsrc(const_cast<void*>(p), 0, bytes, 0x00020000);
}
template <int AUX> __device__ __forceinline__ u32x4 ld16(rsrc_t r, int voff, int soff) { return __builtin_amdgcn_raw_buffer_load_b128(r, voff, soff, AUX); }
__device__ __forceinline__ void st16_sc1(u32x4 v, rsrc_t r, int voff, int off) { __builtin_amdgcn_raw_buffer_store_b128(v, r, voff + off, 0, AUX_SC1); }
__device__ __forceinline__ void st8_sc1(u32x2 v, rsrc_t r, int voff, int soff) { __builtin_amdgcn_raw_buffer_store_b64(v, r, voff, soff, AUX_SC1); }
#define FENCE() asm volatile("" ::: "memory")
#define LDS_DONE() asm volatile("s_waitcnt lgkmcnt(0)" ::: "memory")
#define SCHED_FENCE() __builtin_amdgcn_sched_barrier(0)
template <int N> __device__ __forceinline__ void vm_wait() { asm volatile("s_waitcnt vmcnt(%0)" ::"n"(N) : "memory"); }
__device__ __forceinline__ void vm_wait_n(int n) {
  switch (n) { case 3: vm_wait<3>(); break; case 4: vm_wait<4>(); break; case 5: vm_wait<5>(); break; case 6: vm_wait<6>(); break; default: vm_wait<7>(); }
}
__device__ __forceinline__ u64 now() { return __builtin_amdgcn_s_memrealtime(); }

struct Tag { unsigned a, b; };
__device__ __forceinline__ bool fresh(const u32x4& v, const Tag& t) { return (v[1] == t.a) & (v[3] == t.b); }
__device__ __forceinline__ u32x4 piece(unsigned p0, unsigned p1, const Tag& t) { return u32x4{p0, t.a, p1, t.b}; }
__device__ __forceinline__ u32x4 piece(float p0, float p1, const Tag& t) { return u32x4{__float_as_uint(p0), t.a, __float_as_uint(p1), t.b}; }

template <int N> __device__ __forceinline__ void gather_init(u32x4 (&v)[N], bool act, const Tag& t) {
#pragma unroll
  for (int i = 0; i < N; ++i) v[i] = u32x4{0u, act ? ~t.a : t.a, 0u, t.b};
}
template <int N, class Off> __device__ __forceinline__ bool gather_pass(u32x4 (&v)[N], rsrc_t ws, int voff, const Tag& t, Off off) {
  FENCE();
#pragma unroll
  for (int i = 0; i < N; ++i)
    if (!fresh(v[i], t)) v[i] = ld16<AUX_SC1>(ws, voff, off(i));
  bool ok = true;
#pragma unroll
  for (int i = 0; i < N; ++i) ok &= fresh(v[i], t);
  const bool all = __all(ok);
  FENCE();
  return all;
}
template <int N, class Off> __device__ __forceinline__ void gather_wait(u32x4 (&v)[N], rsrc_t ws, int voff, const Tag& t, Off off) {
  const u64 t0 = now();
  while (!gather_pass(v, ws, voff, t, off)) {
    if (now() - t0 > SPIN_LIMIT) break;
    __builtin_amdgcn_s_sleep(POLL_SLEEP);
  }
}
template <int N, class Off> __device__ __forceinline__ void gather(u32x4 (&v)[N], rsrc_t ws, int voff, bool act, const Tag& t, Off off) {
  gather_init(v, act, t);
  gather_wait(v, ws, voff, t, off);
}
__device__ __forceinline__ u64 epoch_hash(u32x2 e) {
  asm volatile("" : "+v"(e));
  u64 z = (((u64)(unsigned)__builtin_amdgcn_readfirstlane(e[1]) << 32) | (unsigned)__builtin_amdgcn_readfirstlane(e[0])) + 0x9E3779B97F4A7C15ull;
  z = (z ^ (z >> 30)) * 0xBF58476D1CE4E5B9ull; z = (z ^ (z >> 27)) * 0x94D049BB133111EBull;
  return z ^ (z >> 31);
}
__device__ __forceinline__ Tag mk_tag(u64 z) { return Tag{(unsigned)z, (unsigned)(z >> 32)}; }
__device__ __forceinline__ void lds_wait(LAS unsigned* w, unsigned want) {
  const u64 t0 = now();
  while (__hip_atomic_load(w, RLX_WG) != want) {
    if (now() - t0 > SPIN_LIMIT) break;
    __builtin_amdgcn_s_sleep(1);
  }
  FENCE();
}

template <int N, int WIN> __device__ __forceinline__ void tile_load(u32x4 (&R)[N], rsrc_t r, int voff, int voff3, int step, int row) {
#pragma unroll
  for (int i = 0; i < N; ++i) {
    if (i >= WIN) vm_wait<WIN - 1>();
    R[i] = ld16<AUX_NT>(r, i < 24 ? voff : voff3, (i >> 3) * step + (i & 7) * row);
    FENCE();
  }
}
template <int N> __device__ __forceinline__ u32x4 tile_frag(const u32x4 (&R)[N], int ks, int e) {
  u32x4 f;
#pragma unroll
  for (int p = 0; p < 4; ++p)
    f[p] = __builtin_amdgcn_perm(R[ks * 8 + 2 * p + 1][e >> 1], R[ks * 8 + 2 * p][e >> 1], (e & 1) ? 0x07060302u : 0x05040100u);
  return f;
}
__device__ __forceinline__ f32x4 mfma(const u32x4& a, const u32x4& b, const f32x4& c) {
  return __builtin_amdgcn_mfma_f32_16x16x32_bf16(__builtin_bit_cast(bf16x8, a), __builtin_bit_cast(bf16x8, b), c, 0, 0, 0);
}
template <int N> __device__ __forceinline__ void tile_tmfma(f32x4 (&acc)[8], const u32x4 (&A)[N / 8], const u32x4 (&R)[N]) {
#pragma unroll
  for (int e = 0; e < 8; ++e) acc[e] = f32x4{0.f, 0.f, 0.f, 0.f};
#pragma unroll
  for (int ks = 0; ks < N / 8; ++ks) {
#pragma unroll
    for (int e = 0; e < 8; ++e) acc[e] = mfma(A[ks], tile_frag(R, ks, e), acc[e]);
    SCHED_FENCE();
  }
}
__device__ __forceinline__ bool tile64_partial(const f32x4 (&acc)[8], LAS unsigned* part, LAS unsigned* counter, int lane) {
  const int gh = lane >> 5, g1 = (lane >> 4) & 1, ch = (lane >> 3) & 1, c7 = lane & 7;
  float y[2][8];
#pragma unroll
  for (int e = 0; e < 8; ++e)
#pragma unroll
    for (int r = 0; r < 2; ++r) {
      auto s = __builtin_amdgcn_permlane32_swap(__float_as_uint(acc[e][r]), __float_as_uint(acc[e][r + 2]), false, false);
      const float own = gh ? __uint_as_float(s[1]) : __uint_as_float(s[0]), other = gh ? __uint_as_float(s[0]) : __uint_as_float(s[1]);
      y[r][e] = own + dpp<XOR8>(other);
    }
  if (gh == ch) {
#pragma unroll
    for (int r = 0; r < 2; ++r) {
      LAS unsigned* d = part + (4 * g1 + 2 * gh + r) * 64 + c7 * 8;
      *(LAS f32x4*)d = f32x4{y[r][0], y[r][1], y[r][2], y[r][3]};
      *(LAS f32x4*)(d + 4) = f32x4{y[r][4], y[r][5], y[r][6], y[r][7]};
    }
  }
  LDS_DONE();
  unsigned arrived = 0;
  if (lane == 0) arrived = __hip_atomic_fetch_add(counter, 1u, RLX_WG);
  const bool last = __builtin_amdgcn_readfirstlane(arrived) == 3u;
  FENCE();
  return last;
}
__device__ __forceinline__ f32x2 tile64_sum(LAS unsigned* part, int i, int lane) {
  f32x2 s = {0.f, 0.f};
#pragma unroll
  for (int w = 0; w < 4; ++w) s += *(LAS f32x2*)(part + w * 512 + i * 128 + lane * 2);
  return s;
}

struct Args {
  const unsigned short *x, *kc, *vc, *wq, *wo, *wg, *wu, *wd;
  unsigned short* out;
  unsigned char* ws;
};

#define STAMP(i) do {} while (0)
#define STAMP_FLUSH() do {} while (0)

__device__ __forceinline__ float div_by(float a, float d, float rd) {
  const float q = a * rd;
  return fmaf(fmaf(-q, d, a), rd, q);
}
__device__ __forceinline__ void ln_row(const u32x4& x0, const u32x4& x1, LAS unsigned* dst, int lane) {
  float v[16];
#pragma unroll
  for (int k = 0; k < 4; ++k) { v[2 * k] = bf_lo(x0[k]); v[2 * k + 1] = bf_hi(x0[k]); v[8 + 2 * k] = bf_lo(x1[k]); v[8 + 2 * k + 1] = bf_hi(x1[k]); }
  float s1 = 0.f;
#pragma unroll
  for (int k = 0; k < 8; ++k) s1 += v[k] + v[8 + k];
  const float mean = xrow_sum(row16_sum(s1)) * (1.f / D);
  float s2 = 0.f;
#pragma unroll
  for (int k = 0; k < 16; ++k) s2 = fmaf(v[k] - mean, v[k] - mean, s2);
  const float var = xrow_sum(row16_sum(s2)) * (1.f / D);
  const float m = rbf(mean), den = rbf(sqrtf(rbf(rbf(var) + 1e-5f))), rden = __builtin_amdgcn_rcpf(den);
  u32x4 o0, o1;
#pragma unroll
  for (int k = 0; k < 4; ++k) {
    const unsigned d0 = pk_bf16(v[2 * k] - m, v[2 * k + 1] - m), d1 = pk_bf16(v[8 + 2 * k] - m, v[8 + 2 * k + 1] - m);
    o0[k] = pk_bf16(div_by(bf_lo(d0), den, rden), div_by(bf_hi(d0), den, rden));
    o1[k] = pk_bf16(div_by(bf_lo(d1), den, rden), div_by(bf_hi(d1), den, rden));
  }
  *(LAS u32x4*)(dst + lane * 4) = o0;
  *(LAS u32x4*)(dst + 256 + lane * 4) = o1;
}

__global__ void __launch_bounds__(512, 2) fused_decode(Args a) {
  __shared__ __attribute__((aligned(16))) unsigned lds_[L_END];
  LAS unsigned* lds = (LAS unsigned*)lds_;
  asm volatile("" :: "s"(a.x), "s"(a.kc), "s"(a.vc), "s"(a.wq), "s"(a.wo), "s"(a.wg), "s"(a.wu), "s"(a.wd), "s"(a.out), "s"(a.ws));
  const int lane = threadIdx.x & 63, wave = __builtin_amdgcn_readfirstlane(threadIdx.x >> 6);
  const int g = lane >> 4, c = lane & 15, gh = g >> 1, g1 = g & 1, c7 = c & 7, ch = c >> 3;
  const int wg = blockIdx.x, b = wg >> 5, h = wg & 31;
  unsigned stampv = 0; (void)stampv;
  const rsrc_t ws = mk_rsrc(a.ws, WS_END);
  u32x2 ep;

  if (wave == 0) STAMP(0);
  const u64 t_entry = now();
  const bool t_ffn = h < 14, t_q = h >= 16;
  const int u = wave - NS;
  const int tb = (b + TROT) & 7;
  const int tix = t_ffn ? h * 8 + tb : (h - 16) * 8 + tb;
  const int ld2 = (t_ffn ? F : H * DH) * 2;
  const int k0 = t_ffn ? 256 * (u & 3) : 512 * ((tix >> 6) & 1) + 128 * (u & 3);
  const rsrc_t rw1 = mk_rsrc((const char*)(t_ffn ? (tix < NFT ? a.wg : a.wu) : a.wq) + (size_t)k0 * ld2 + (t_ffn ? (tix < NFT ? tix : tix - NFT) : (tix & 63)) * 128,
                             (wave >= NS && (t_ffn | t_q) && !(ABL & 1)) ? 256 * ld2 : 0);
  const int voff1 = (32 * ch + 8 * g) * ld2 + c7 * 16;
  u32x4 R0[8];
  {
    const rsrc_t rx = mk_rsrc(a.x, B * D * 2);
#pragma unroll
    for (int i = 0; i < 8; ++i) {
      if (wave < NS) { if (i < 4) R0[i] = ld16<0>(rx, wave * 2048 + lane * 16, (i & 1) * 1024 + (i >> 1) * 8192); }
      else R0[i] = ld16<AUX_NT>(rw1, voff1, i * ld2);
    }
    ep = __builtin_amdgcn_raw_buffer_load_b64(ws, WS_EPOCH, 0, 0);
    FENCE();
    if (wave == 0) { if (lane < 8) lds[L_FX + lane] = 0u; LDS_DONE(); }
    __builtin_amdgcn_s_barrier();
    FENCE();
  }

  if (wave < NS) {
    const rsrc_t rk = mk_rsrc(a.kc + (size_t)wg * SK * DH, SK * DH * 2);
    const rsrc_t rv = mk_rsrc(a.vc + (size_t)wg * SK * DH, SK * DH * 2);
    const int kvoff = lane * 16 + wave * T * 1024;
    if (t_ffn | t_q) {
#pragma unroll
      for (int i = 0; i < 2; ++i) ln_row(R0[2 * i], R0[2 * i + 1], lds + L_XN + (wave + 4 * i) * XN_STRIDE, lane);
      LDS_DONE();
      if (lane == 0) __hip_atomic_fetch_add(lds + L_FX, 1u, RLX_WG);
      FENCE();
    }
    if ((t_ffn | t_q) && !(ABL & 1)) {
      const u64 limit = t_q ? SPIN_LIMIT : KV_T0;
      while (__hip_atomic_load(lds + L_FP, RLX_WG) != 4u && now() - t_entry < limit) __builtin_amdgcn_s_sleep(2);
      FENCE();
    }
    u32x4 Kr[T], Vr[T], X[NX + 1][2 * T];
#pragma unroll
    for (int i = 0; i < 2 * T; ++i) {
      if (i >= WIN_K) vm_wait<(WIN_K > 0 ? WIN_K - 1 : 0)>();
      (i < T ? Kr[i] : Vr[i - T]) = ld16<AUX_NT>(i < T ? rk : rv, kvoff + (i % T) * 1024, 0);
      FENCE();
    }
    int nx = 0;
    {
      const Tag tag = mk_tag(epoch_hash(ep));
      u32x4 v[2];
      gather_init(v, wave == 0, tag);
      auto qoff = [](int i) { return i * 1024; };
      const int qo = WS_Q + wg * 2048 + lane * 16;
      bool have = ABL & 1;
#pragma unroll
      for (int j = 0; j < NX; ++j) {
        if (!have) {
#pragma unroll
          for (int i = 0; i < 2 * T; ++i) {
            vm_wait<WIN_K - 1>();
            X[j][i] = ld16<AUX_NT>(i < T ? rk : rv, kvoff + (i % T) * 1024, (j + 1) * NS * T * 1024);
            FENCE();
          }
          nx = j + 1;
          have = wave == 0 ? gather_pass(v, ws, qo, tag, qoff) : __hip_atomic_load(lds + L_FQ, RLX_WG) == 1u;
        }
      }
      if (ABL & 1) {
      } else if (wave == 0) {
        if (!have) gather_wait(v, ws, qo, tag, qoff);
        lds[L_Q + lane] = pk_bf16(__uint_as_float(v[0][0]) + __uint_as_float(v[1][0]), __uint_as_float(v[0][2]) + __uint_as_float(v[1][2]));
        LDS_DONE();
        if (lane == 0) __hip_atomic_store(lds + L_FQ, 1u, RLX_WG);
        FENCE();
        STAMP(3);
      } else {
        lds_wait(lds + L_FQ, 1u);
      }
    }
    const u32x4 qv = *(LAS u32x4*)(lds + L_Q + c * 4);
    constexpr float SCALE = 0.08838834764831845f, LOG2E = 1.4426950408889634f;
    float m = -INFINITY, l = 0.f, acc[8];
#pragma unroll
    for (int k = 0; k < 8; ++k) acc[k] = 0.f;
    auto step = [&](u32x4* Kt, u32x4* Vt, const bool more, const int so) __attribute__((always_inline)) {
      float s[T];
#pragma unroll
      for (int i = 0; i < T; ++i) {
        float d = 0.f;
#pragma unroll
        for (int k = 0; k < 4; ++k) d = dot2(Kt[i][k], qv[k], d);
        asm volatile("" : "+v"(d));
        s[i] = d;
        SCHED_FENCE();
        if (more) { if (WIN_KK(i) < 2 * T) vm_wait_n(WIN_KK(i) - 1); Kt[i] = ld16<AUX_NT>(rk, kvoff + i * 1024, so); FENCE(); }
        SCHED_FENCE();
      }
      float mx = m;
#pragma unroll
      for (int i = 0; i < T; ++i) {
        s[i] = rbf(rbf(row16_sum(s[i])) * SCALE) * LOG2E;
        mx = fmaxf(mx, s[i]);
      }
      const float alpha = __builtin_amdgcn_exp2f(m - mx);
      m = mx; l *= alpha;
#pragma unroll
      for (int k = 0; k < 8; ++k) acc[k] *= alpha;
#pragma unroll
      for (int i = 0; i < T; ++i) { s[i] = __builtin_amdgcn_exp2f(s[i] - mx); l += s[i]; }
      SCHED_FENCE();
#pragma unroll
      for (int i = 0; i < T; ++i) {
#pragma unroll
        for (int k = 0; k < 4; ++k) { acc[2 * k] = fmaf(s[i], bf_lo(Vt[i][k]), acc[2 * k]); acc[2 * k + 1] = fmaf(s[i], bf_hi(Vt[i][k]), acc[2 * k + 1]); }
#pragma unroll
        for (int k = 0; k < 8; k += 2) { f32x2 p2 = {acc[k], acc[k + 1]}; asm volatile("" : "+v"(p2)); acc[k] = p2[0]; acc[k + 1] = p2[1]; }
        SCHED_FENCE();
        if (more) { if (WIN_VV(i) < 2 * T) vm_wait_n(WIN_VV(i) - 1); Vt[i] = ld16<AUX_NT>(rv, kvoff + i * 1024, so); FENCE(); }
        SCHED_FENCE();
      }
    };
    step(Kr, Vr, true, (nx + 1) * NS * T * 1024);
#pragma unroll
    for (int j = 0; j < NX; ++j)
      if (j < nx) step(&X[j][0], &X[j][T], false, 0);
    for (int tt = nx + 2; tt < NT; ++tt) step(Kr, Vr, true, tt * NS * T * 1024);
    step(Kr, Vr, false, 0);
    STAMP(4 + wave);
    const float M = xrow_max(m), f = __builtin_amdgcn_exp2f(m - M);
    l = xrow_sum(l * f);
#pragma unroll
    for (int k = 0; k < 8; ++k) acc[k] = xrow_sum(acc[k] * f);
    LAS unsigned* rec = lds + L_REC + wave * REC_STRIDE;
    if (g == 0) {
      *(LAS f32x4*)(rec + 8 * c) = f32x4{acc[0], acc[1], acc[2], acc[3]};
      *(LAS f32x4*)(rec + 8 * c + 4) = f32x4{acc[4], acc[5], acc[6], acc[7]};
      if (c == 0) *(LAS f32x2*)(rec + 128) = f32x2{M, l};
    }
    LDS_DONE();
    if (lane == 0) __hip_atomic_fetch_add(lds + L_FS, 1u, RLX_WG);
    STAMP_FLUSH();
    return;
  }

  if (ABL & 1) return;
  if (t_q) {
    u32x4 R[16];
#pragma unroll
    for (int i = 0; i < 8; ++i) { R[i] = R0[i]; vm_wait<WIN_Q - 1>(); R[8 + i] = ld16<AUX_NT>(rw1, voff1, (64 + i) * ld2); FENCE(); }
    lds_wait(lds + L_FX, (unsigned)NS);
    u32x4 A[2]; f32x4 acc[8];
#pragma unroll
    for (int ks = 0; ks < 2; ++ks) A[ks] = *(LAS u32x4*)(lds + L_XN + c7 * XN_STRIDE + (k0 >> 1) + ks * 32 + ch * 16 + g * 4);
    tile_tmfma(acc, A, R);
    if (tile64_partial(acc, lds + L_P1 + u * 512, lds + L_FP, lane)) {
      const Tag tag = mk_tag(epoch_hash(ep));
#pragma unroll
      for (int i = 0; i < 4; ++i) {
        const f32x2 s = tile64_sum(lds + L_P1, i, lane);
        st16_sc1(piece(s[0], s[1], tag), ws,
                 WS_Q + (gh * 32 + ((tix & 63) >> 1)) * 2048 + (tix >> 6) * 1024 + (tix & 1) * 512 + (lane & 31) * 16, i * 2 * 32 * 2048);
      }
      FENCE();
      STAMP(9);
    }
  } else if (t_ffn) {
    u32x4 R[32];
#pragma unroll
    for (int i = 0; i < 8; ++i) R[i] = R0[i];
#pragma unroll
    for (int i = 8; i < 32; ++i) { vm_wait<WIN_F - 1>(); R[i] = ld16<AUX_NT>(rw1, voff1, ((i >> 3) * 64 + (i & 7)) * ld2); FENCE(); }
    if (wave == 4) STAMP(34);
    lds_wait(lds + L_FX, (unsigned)NS);
    u32x4 A[4]; f32x4 acc[8];
#pragma unroll
    for (int ks = 0; ks < 4; ++ks) A[ks] = *(LAS u32x4*)(lds + L_XN + c7 * XN_STRIDE + (k0 >> 1) + ks * 32 + ch * 16 + g * 4);
    tile_tmfma(acc, A, R);
    if (tile64_partial(acc, lds + L_P1 + u * 512, lds + L_FP, lane)) {
      const Tag tag = mk_tag(epoch_hash(ep));
#pragma unroll
      for (int i = 0; i < 2; ++i) {
        f32x4 s = {0.f, 0.f, 0.f, 0.f};
#pragma unroll
        for (int w = 0; w < 4; ++w) s += *(LAS f32x4*)(lds + L_P1 + w * 512 + i * 256 + lane * 4);
        st16_sc1(piece(pk_bf16(s[0], s[1]), pk_bf16(s[2], s[3]), tag), ws,
                 (tix < NFT ? WS_G + tix * 256 : WS_U + (tix - NFT) * 256) + (g * F + c * 4) * 4, i * 4 * F * 4);
      }
      FENCE();
      STAMP(10);
    }
  }
  if (wave == 7 || (wave == 6 && !t_q)) { STAMP_FLUSH(); return; }

  u32x4 W[16];
  f32x4 acc[4];
  const int voff = (8 * g * D + 8 * c) * 2;
  u64 z;
  if (wave >= 5) {
    f32x4 a8[8];
    z = 0;
    if (t_q) {
      const int f0 = (2 * (h - 16) + (wave - 5)) * FS;
      LAS unsigned* ai = lds + L_AI + (wave - 5) * 8 * AI_STRIDE;
      u32x4 R[32];
      const rsrc_t rw = mk_rsrc(a.wd + (size_t)f0 * D + b * 128, FS * D * 2);
      tile_load<32, WIN_S>(R, rw, voff, g >= 2 ? 0x7ffffff0 : voff, 32 * D * 2, D * 2);
      if (wave == 5) STAMP(19);
      const Tag tag = mk_tag(epoch_hash(ep));
      u32x4 v[8];
      gather(v, ws, WS_G + (gh * F + f0 + (lane & 31) * 4) * 4, (lane & 31) < FS / 4, tag, [](int i) { return i * 2 * F * 4; });
      if (wave == 5) STAMP(21);
#pragma unroll
      for (int i = 0; i < 4; ++i) {
        const u32x4 gt = v[i], up = v[4 + i];
        *(LAS u32x2*)(ai + (2 * i + gh) * AI_STRIDE + (lane & 31) * 2) =
            u32x2{pk_bf16(bf_lo(gt[0]) * bf_lo(up[0]), bf_hi(gt[0]) * bf_hi(up[0])), pk_bf16(bf_lo(gt[2]) * bf_lo(up[2]), bf_hi(gt[2]) * bf_hi(up[2]))};
      }
      LDS_DONE();
      u32x4 A[4];
#pragma unroll
      for (int ks = 0; ks < 4; ++ks) A[ks] = *(LAS u32x4*)(ai + c7 * AI_STRIDE + ks * 16 + g * 4);
      tile_tmfma(a8, A, R);
      if (wave == 6) {
#pragma unroll
        for (int e = 0; e < 8; ++e) *(LAS f32x4*)(lds + L_A6 + e * 256 + lane * 4) = a8[e];
        LDS_DONE();
        if (lane == 0) __hip_atomic_store(lds + L_F6, 1u, RLX_WG);
        STAMP_FLUSH();
        return;
      }
      lds_wait(lds + L_F6, 1u);
#pragma unroll
      for (int e = 0; e < 8; ++e) a8[e] += *(LAS f32x4*)(lds + L_A6 + e * 256 + lane * 4);
    } else {
#pragma unroll
      for (int e = 0; e < 8; ++e) a8[e] = f32x4{0.f, 0.f, 0.f, 0.f};
    }
    z = epoch_hash(ep);
#pragma unroll
    for (int e = 0; e < 4; ++e) { *(LAS f32x4*)(lds + L_ACC + e * 256 + lane * 4) = a8[e]; acc[e] = a8[4 + e]; }
    LDS_DONE();
    if (lane == 0) __hip_atomic_store(lds + L_FA, 1u, RLX_WG);
    lds_wait(lds + L_FW, 1u);
#pragma unroll
    for (int i = 0; i < 16; ++i) { W[i] = *(LAS u32x4*)(lds + L_WO + i * 256 + lane * 4); asm volatile("" : "+v"(W[i])); }
    STAMP(22);
    __builtin_amdgcn_s_setprio(2);
    lds_wait(lds + L_FO, 1u);
  } else {
    {
      u32x4 R[32];
      tile_load<32, WIN_S>(R, mk_rsrc(a.wo + (size_t)h * 128 * D + b * 128, 128 * D * 2), voff, voff, 32 * D * 2, D * 2);
#pragma unroll
      for (int ks = 0; ks < 4; ++ks)
#pragma unroll
        for (int e = 0; e < 4; ++e) {
          W[ks * 4 + e] = tile_frag(R, ks, e);
          asm volatile("" : "+v"(W[ks * 4 + e]));
          *(LAS u32x4*)(lds + L_WO + (ks * 4 + e) * 256 + lane * 4) = tile_frag(R, ks, 4 + e);
        }
      LDS_DONE();
      if (lane == 0) __hip_atomic_store(lds + L_FW, 1u, RLX_WG);
    }
    z = epoch_hash(ep);
    const Tag tag = mk_tag(z);
    STAMP(13);
    lds_wait(lds + L_FA, 1u);
#pragma unroll
    for (int e = 0; e < 4; ++e) { acc[e] = *(LAS f32x4*)(lds + L_ACC + e * 256 + lane * 4); asm volatile("" : "+v"(acc[e])); }
    __builtin_amdgcn_s_setprio(2);
    lds_wait(lds + L_FS, (unsigned)NS);
    STAMP(14);
    float mw[NS], lw[NS]; f32x2 aw[NS];
#pragma unroll
    for (int w = 0; w < NS; ++w) {
      const f32x2 ml = *(LAS f32x2*)(lds + L_REC + w * REC_STRIDE + 128);
      mw[w] = ml[0]; lw[w] = ml[1];
      aw[w] = *(LAS f32x2*)(lds + L_REC + w * REC_STRIDE + 2 * lane);
    }
    float M = mw[0];
#pragma unroll
    for (int w = 1; w < NS; ++w) M = fmaxf(M, mw[w]);
    float L = 0.f, o0 = 0.f, o1 = 0.f;
#pragma unroll
    for (int w = 0; w < NS; ++w) { const float f = __builtin_amdgcn_exp2f(mw[w] - M); L = fmaf(f, lw[w], L); o0 = fmaf(f, aw[w][0], o0); o1 = fmaf(f, aw[w][1], o1); }
    const float inv = __builtin_amdgcn_rcpf(L);
    const unsigned op = pk_bf16(div_by(o0, L, inv), div_by(o1, L, inv)), on = dppu<XOR1>(op);
    if ((lane & 1) == 0) st16_sc1(piece(op, on, tag), ws, WS_O + (h * 8 + b) * 512 + lane * 8, 0);
    STAMP(15);
    u32x4 v[4];
    gather(v, ws, WS_O + h * 4096 + lane * 16, true, tag, [](int i) { return i * 1024; });
    STAMP(16);
#pragma unroll
    for (int i = 0; i < 4; ++i) *(LAS u32x2*)(lds + L_AI + (2 * i + gh) * AI_STRIDE + (lane & 31) * 2) = u32x2{v[i][0], v[i][2]};
    LDS_DONE();
    if (lane == 0) __hip_atomic_store(lds + L_FO, 1u, RLX_WG);
    FENCE();
  }
  const Tag tag = mk_tag(z);
#pragma unroll
  for (int ks = 0; ks < 4; ++ks) {
    const u32x4 A = *(LAS u32x4*)(lds + L_AI + c7 * AI_STRIDE + ks * 16 + g * 4);
#pragma unroll
    for (int e = 0; e < 4; ++e) acc[e] = mfma(A, W[ks * 4 + e], acc[e]);
  }
#pragma unroll
  for (int r = 0; r < 4; ++r) {
    const float v0 = gh ? acc[2][r] : acc[0][r], v1 = gh ? acc[3][r] : acc[1][r];
    st16_sc1(piece(v0, v1, tag), ws,
             WS_P + ((b * 32 + 4 * g1 * 4 + (c >> 2)) * 32 + h) * 256 + u * 128 + ((c & 3) * 2 + gh) * 16, r * 4 * 32 * 256);
  }
  if (wave == 5) { STAMP_FLUSH(); return; }
  STAMP(17);
  {
    const int col = b * 128 + (h & 3) * 32 + (c7 >> 1) * 8 + ch * 4 + (c & 1) * 2;
    const unsigned xres = *(const GAS unsigned*)(a.x + (h >> 2) * D + col);
    u32x4 p[8];
    gather(p, ws, WS_P + (b * 32 + h) * 8192 + lane * 16, true, tag, [](int i) { return i * 1024; });
    float y0 = 0.f, y1 = 0.f;
#pragma unroll
    for (int i = 0; i < 8; ++i) { y0 += __uint_as_float(p[i][0]); y1 += __uint_as_float(p[i][2]); }
    y0 = xrow_sum(y0); y1 = xrow_sum(y1);
    if (g == 0) *(GAS unsigned*)(a.out + (h >> 2) * D + col) = pk_bf16(bf_lo(xres) + y0, bf_hi(xres) + y1);
  }
  if (wg == 0 && lane == 0) st8_sc1(u32x2{(unsigned)z, (unsigned)(z >> 32)}, ws, WS_EPOCH, 0);
  STAMP(18);
  STAMP_FLUSH();
}

}

extern "C" void kernel_launch(void* const* d_in, const int* in_sizes, int n_in, void* d_out, int out_size, void* d_ws, size_t ws_size,
                              hipStream_t stream) {
  Args a;
  a.x = (const unsigned short*)d_in[0];
  a.kc = (const unsigned short*)d_in[1];
  a.vc = (const unsigned short*)d_in[2];
  a.wq = (const unsigned short*)d_in[3];
  a.wo = (const unsigned short*)d_in[4];
  a.wg = (const unsigned short*)d_in[6];
  a.wu = (const unsigned short*)d_in[7];
  a.wd = (const unsigned short*)d_in[8];
  a.out = (unsigned short*)d_out;
  a.ws = (unsigned char*)d_ws;
  fused_decode<<<B * H, 512, 0, stream>>>(a);
}
```

```cpp
#include <hip/hip_runtime.h>
#include <hip/hip_bf16.h>
#include <stdint.h>

#define STAMPS 0
#define KV_T 4
#define POLL_SLEEP 2
#define WIN_Q 8
#define WIN_F 8
#define WIN_S 6
#define PRE 0
#define NX 0
#define WIN_K 8
#define WIN_KV 5
#define ABL 0
#define TROT 0
#define KV_T0 420
#define WMASK 0
#define WIN_KK(i) (WIN_KV + ((WMASK >> (i)) & 1))
#define WIN_VV(i) (WIN_KV + ((WMASK >> (4 + (i))) & 1))
#define TOUCH 1
#define CODE_PF 0

namespace {

typedef unsigned u32x4 __attribute__((ext_vector_type(4)));
typedef unsigned u32x2 __attribute__((ext_vector_type(2)));
typedef float f32x4 __attribute__((ext_vector_type(4)));
typedef float f32x2 __attribute__((ext_vector_type(2)));
typedef __bf16 bf16x2 __attribute__((ext_vector_type(2)));
typedef __bf16 bf16x8 __attribute__((ext_vector_type(8)));
typedef unsigned long long u64;
#define GAS __attribute__((address_space(1)))
#define LAS __attribute__((address_space(3)))
#define RLX_WG __ATOMIC_RELAXED, __HIP_MEMORY_SCOPE_WORKGROUP
typedef __amdgpu_buffer_rsrc_t rsrc_t;

constexpr int B = 8, D = 1024, H = 32, SK = 4096, DH = 128, F = 3584;
constexpr int FS = F / 32;
constexpr int NQT = H * DH / 64, NFT = F / 64;
constexpr int AUX_NT = 2, AUX_SC1 = 16;
constexpr int NS = 4, T = KV_T;
constexpr int NT = (SK / 4) / (NS * T);

constexpr int WS_EPOCH = 0;
constexpr int WS_Q = 4096;
constexpr int WS_G = WS_Q + B * H * 2 * DH * 8;
constexpr int WS_U = WS_G + B * F * 4;
constexpr int WS_O = WS_U + B * F * 4;
constexpr int WS_P = WS_O + B * H * DH * 4;
constexpr int WS_END = WS_P + 256 * 8192;
constexpr int WS_STAMP = 16 << 20;

constexpr u64 SPIN_LIMIT = 2000000;

constexpr int XN_STRIDE = 516;
constexpr int L_XN = 0;
constexpr int L_P1 = L_XN + 8 * XN_STRIDE;
constexpr int L_A6 = L_P1 + 2048;
constexpr int L_ACC = L_A6 + 2048;
constexpr int L_WO = L_ACC + 1024;
constexpr int AI_STRIDE = 68;
constexpr int L_AI = L_WO + 4096;
constexpr int L_Q = L_AI + 16 * AI_STRIDE;
constexpr int L_REC = L_Q + 64;
constexpr int REC_STRIDE = 132;
constexpr int L_FX = L_REC + NS * REC_STRIDE;
constexpr int L_FP = L_FX + 1, L_FQ = L_FX + 2, L_FS = L_FX + 3, L_FA = L_FX + 4, L_FW = L_FX + 5, L_FO = L_FX + 6, L_F6 = L_FX + 7;
constexpr int L_END = L_FX + 8;

__device__ __forceinline__ float bf_lo(unsigned u) { return __uint_as_float(u << 16); }
__device__ __forceinline__ float bf_hi(unsigned u) { return __uint_as_float(u & 0xffff0000u); }
__device__ __forceinline__ unsigned pk_bf16(float lo, float hi) {
  const f32x2 v = {lo, hi};
  return __builtin_bit_cast(unsigned, __builtin_convertvector(v, bf16x2));
}
__device__ __forceinline__ float rbf(float x) { return bf_lo(pk_bf16(x, x)); }
__device__ __forceinline__ float dot2(unsigned k, unsigned q, float acc) {
  return __builtin_amdgcn_fdot2_f32_bf16(__builtin_bit_cast(bf16x2, k), __builtin_bit_cast(bf16x2, q), acc, false);
}
template <int CTRL> __device__ __forceinline__ unsigned dppu(unsigned x) { return (unsigned)__builtin_amdgcn_mov_dpp((int)x, CTRL, 0xf, 0xf, true); }
template <int CTRL> __device__ __forceinline__ float dpp(float x) { return __uint_as_float(dppu<CTRL>(__float_as_uint(x))); }
constexpr int XOR1 = 0xB1, XOR2 = 0x4E, XOR7 = 0x141, XOR8 = 0x128;
__device__ __forceinline__ float row16_sum(float d) {
  d += dpp<XOR1>(d); d += dpp<XOR2>(d); d += dpp<XOR7>(d); d += dpp<XOR8>(d);
  return d;
}
__device__ __forceinline__ float xrow_sum(float x) {
  auto s = __builtin_amdgcn_permlane16_swap(__float_as_uint(x), __float_as_uint(x), false, false);
  x = __uint_as_float(s[0]) + __uint_as_float(s[1]);
  auto t = __builtin_amdgcn_permlane32_swap(__float_as_uint(x), __float_as_uint(x), false, false);
  return __uint_as_float(t[0]) + __uint_as_float(t[1]);
}
__device__ __forceinline__ float xrow_max(float x) {
  auto s = __builtin_amdgcn_permlane16_swap(__float_as_uint(x), __float_as_uint(x), false, false);
  x = fmaxf(__uint_as_float(s[0]), __uint_as_float(s[1]));
  auto t = __builtin_amdgcn_permlane32_swap(__float_as_uint(x), __float_as_uint(x), false, false);
  return fmaxf(__uint_as_float(t[0]), __uint_as_float(t[1]));
}
__device__ __forceinline__ rsrc_t mk_rsrc(const void* p, unsigned bytes) {
  return __builtin_amdgcn_make_buffer_rsrc(const_cast<void*>(p), 0, bytes, 0x00020000);
}
template <int AUX> __device__ __forceinline__ u32x4 ld16(rsrc_t r, int voff, int soff) { return __builtin_amdgcn_raw_buffer_load_b128(r, voff, soff, AUX); }
__device__ __forceinline__ void st16_sc1(u32x4 v, rsrc_t r, int voff, int off) { __builtin_amdgcn_raw_buffer_store_b128(v, r, voff + off, 0, AUX_SC1); }
__device__ __forceinline__ void st8_sc1(u32x2 v, rsrc_t r, int voff, int soff) { __builtin_amdgcn_raw_buffer_store_b64(v, r, voff, soff, AUX_SC1); }
#define FENCE() asm volatile("" ::: "memory")
#define LDS_DONE() asm volatile("s_waitcnt lgkmcnt(0)" ::: "memory")
#define SCHED_FENCE() __builtin_amdgcn_sched_barrier(0)
template <int N> __device__ __forceinline__ void vm_wait() { asm volatile("s_waitcnt vmcnt(%0)" ::"n"(N) : "memory"); }
__device__ __forceinline__ void vm_wait_n(int n) {
  switch (n) { case 3: vm_wait<3>(); break; case 4: vm_wait<4>(); break; case 5: vm_wait<5>(); break; case 6: vm_wait<6>(); break; default: vm_wait<7>(); }
}
__device__ __forceinline__ u64 now() { return __builtin_amdgcn_s_memrealtime(); }

struct Tag { unsigned a, b; };
__device__ __forceinline__ bool fresh(const u32x4& v, const Tag& t) { return (v[1] == t.a) & (v[3] == t.b); }
__device__ __forceinline__ u32x4 piece(unsigned p0, unsigned p1, const Tag& t) { return u32x4{p0, t.a, p1, t.b}; }
__device__ __forceinline__ u32x4 piece(float p0, float p1, const Tag& t) { return u32x4{__float_as_uint(p0), t.a, __float_as_uint(p1), t.b}; }

template <int N> __device__ __forceinline__ void gather_init(u32x4 (&v)[N], bool act, const Tag& t) {
#pragma unroll
  for (int i = 0; i < N; ++i) v[i] = u32x4{0u, act ? ~t.a : t.a, 0u, t.b};
}
template <int N, class Off> __device__ __forceinline__ bool gather_pass(u32x4 (&v)[N], rsrc_t ws, int voff, const Tag& t, Off off) {
  FENCE();
#pragma unroll
  for (int i = 0; i < N; ++i)
    if (!fresh(v[i], t)) v[i] = ld16<AUX_SC1>(ws, voff, off(i));
  bool ok = true;
#pragma unroll
  for (int i = 0; i < N; ++i) ok &= fresh(v[i], t);
  const bool all = __all(ok);
  FENCE();
  return all;
}
template <int N, class Off> __device__ __forceinline__ void gather_wait(u32x4 (&v)[N], rsrc_t ws, int voff, const Tag& t, Off off) {
  const u64 t0 = now();
  while (!gather_pass(v, ws, voff, t, off)) {
    if (now() - t0 > SPIN_LIMIT) break;
    __builtin_amdgcn_s_sleep(POLL_SLEEP);
  }
}
template <int N, class Off> __device__ __forceinline__ void gather(u32x4 (&v)[N], rsrc_t ws, int voff, bool act, const Tag& t, Off off) {
  gather_init(v, act, t);
  gather_wait(v, ws, voff, t, off);
}
__device__ __forceinline__ u64 epoch_hash(u32x2 e) {
  asm volatile("" : "+v"(e));
  u64 z = (((u64)(unsigned)__builtin_amdgcn_readfirstlane(e[1]) << 32) | (unsigned)__builtin_amdgcn_readfirstlane(e[0])) + 0x9E3779B97F4A7C15ull;
  z = (z ^ (z >> 30)) * 0xBF58476D1CE4E5B9ull; z = (z ^ (z >> 27)) * 0x94D049BB133111EBull;
  return z ^ (z >> 31);
}
__device__ __forceinline__ Tag mk_tag(u64 z) { return Tag{(unsigned)z, (unsigned)(z >> 32)}; }
__device__ __forceinline__ void lds_wait(LAS unsigned* w, unsigned want) {
  const u64 t0 = now();
  while (__hip_atomic_load(w, RLX_WG) != want) {
    if (now() - t0 > SPIN_LIMIT) break;
    __builtin_amdgcn_s_sleep(1);
  }
  FENCE();
}

template <int N, int WIN> __device__ __forceinline__ void tile_load(u32x4 (&R)[N], rsrc_t r, int voff, int voff3, int step, int row) {
#pragma unroll
  for (int i = 0; i < N; ++i) {
    if (i >= WIN) vm_wait<WIN - 1>();
    R[i] = ld16<AUX_NT>(r, i < 24 ? voff : voff3, (i >> 3) * step + (i & 7) * row);
    FENCE();
  }
}
template <int N> __device__ __forceinline__ u32x4 tile_frag(const u32x4 (&R)[N], int ks, int e) {
  u32x4 f;
#pragma unroll
  for (int p = 0; p < 4; ++p)
    f[p] = __builtin_amdgcn_perm(R[ks * 8 + 2 * p + 1][e >> 1], R[ks * 8 + 2 * p][e >> 1], (e & 1) ? 0x07060302u : 0x05040100u);
  return f;
}
__device__ __forceinline__ f32x4 mfma(const u32x4& a, const u32x4& b, const f32x4& c) {
  return __builtin_amdgcn_mfma_f32_16x16x32_bf16(__builtin_bit_cast(bf16x8, a), __builtin_bit_cast(bf16x8, b), c, 0, 0, 0);
}
template <int N> __device__ __forceinline__ void tile_tmfma(f32x4 (&acc)[8], const u32x4 (&A)[N / 8], const u32x4 (&R)[N]) {
#pragma unroll
  for (int e = 0; e < 8; ++e) acc[e] = f32x4{0.f, 0.f, 0.f, 0.f};
#pragma unroll
  for (int ks = 0; ks < N / 8; ++ks) {
#pragma unroll
    for (int e = 0; e < 8; ++e) acc[e] = mfma(A[ks], tile_frag(R, ks, e), acc[e]);
    SCHED_FENCE();
  }
}
__device__ __forceinline__ bool tile64_partial(const f32x4 (&acc)[8], LAS unsigned* part, LAS unsigned* counter, int lane) {
  const int gh = lane >> 5, g1 = (lane >> 4) & 1, ch = (lane >> 3) & 1, c7 = lane & 7;
  float y[2][8];
#pragma unroll
  for (int e = 0; e < 8; ++e)
#pragma unroll
    for (int r = 0; r < 2; ++r) {
      auto s = __builtin_amdgcn_permlane32_swap(__float_as_uint(acc[e][r]), __float_as_uint(acc[e][r + 2]), false, false);
      const float own = gh ? __uint_as_float(s[1]) : __uint_as_float(s[0]), other = gh ? __uint_as_float(s[0]) : __uint_as_float(s[1]);
      y[r][e] = own + dpp<XOR8>(other);
    }
  if (gh == ch) {
#pragma unroll
    for (int r = 0; r < 2; ++r) {
      LAS unsigned* d = part + (4 * g1 + 2 * gh + r) * 64 + c7 * 8;
      *(LAS f32x4*)d = f32x4{y[r][0], y[r][1], y[r][2], y[r][3]};
      *(LAS f32x4*)(d + 4) = f32x4{y[r][4], y[r][5], y[r][6], y[r][7]};
    }
  }
  LDS_DONE();
  unsigned arrived = 0;
  if (lane == 0) arrived = __hip_atomic_fetch_add(counter, 1u, RLX_WG);
  const bool last = __builtin_amdgcn_readfirstlane(arrived) == 3u;
  FENCE();
  return last;
}
__device__ __forceinline__ f32x2 tile64_sum(LAS unsigned* part, int i, int lane) {
  f32x2 s = {0.f, 0.f};
#pragma unroll
  for (int w = 0; w < 4; ++w) s += *(LAS f32x2*)(part + w * 512 + i * 128 + lane * 2);
  return s;
}

struct Args {
  const unsigned short *x, *kc, *vc, *wq, *wo, *wg, *wu, *wd;
  unsigned short* out;
  unsigned char* ws;
};

#define STAMP(i) do {} while (0)
#define STAMP_FLUSH() do {} while (0)

__device__ __forceinline__ float div_by(float a, float d, float rd) {
  const float q = a * rd;
  return fmaf(fmaf(-q, d, a), rd, q);
}
__device__ __forceinline__ void ln_row(const u32x4& x0, const u32x4& x1, LAS unsigned* dst, int lane) {
  float v[16];
#pragma unroll
  for (int k = 0; k < 4; ++k) { v[2 * k] = bf_lo(x0[k]); v[2 * k + 1] = bf_hi(x0[k]); v[8 + 2 * k] = bf_lo(x1[k]); v[8 + 2 * k + 1] = bf_hi(x1[k]); }
  float s1 = 0.f;
#pragma unroll
  for (int k = 0; k < 8; ++k) s1 += v[k] + v[8 + k];
  const float mean = xrow_sum(row16_sum(s1)) * (1.f / D);
  float s2 = 0.f;
#pragma unroll
  for (int k = 0; k < 16; ++k) s2 = fmaf(v[k] - mean, v[k] - mean, s2);
  const float var = xrow_sum(row16_sum(s2)) * (1.f / D);
  const float m = rbf(mean), den = rbf(sqrtf(rbf(rbf(var) + 1e-5f))), rden = __builtin_amdgcn_rcpf(den);
  u32x4 o0, o1;
#pragma unroll
  for (int k = 0; k < 4; ++k) {
    const unsigned d0 = pk_bf16(v[2 * k] - m, v[2 * k + 1] - m), d1 = pk_bf16(v[8 + 2 * k] - m, v[8 + 2 * k + 1] - m);
    o0[k] = pk_bf16(div_by(bf_lo(d0), den, rden), div_by(bf_hi(d0), den, rden));
    o1[k] = pk_bf16(div_by(bf_lo(d1), den, rden), div_by(bf_hi(d1), den, rden));
  }
  *(LAS u32x4*)(dst + lane * 4) = o0;
  *(LAS u32x4*)(dst + 256 + lane * 4) = o1;
}

__global__ void __launch_bounds__(512, 2) fused_decode(Args a) {
  __shared__ __attribute__((aligned(16))) unsigned lds_[L_END];
  LAS unsigned* lds = (LAS unsigned*)lds_;
  asm volatile("" :: "s"(a.x), "s"(a.kc), "s"(a.vc), "s"(a.wq), "s"(a.wo), "s"(a.wg), "s"(a.wu), "s"(a.wd), "s"(a.out), "s"(a.ws));
  const int lane = threadIdx.x & 63, wave = __builtin_amdgcn_readfirstlane(threadIdx.x >> 6);
  const int g = lane >> 4, c = lane & 15, gh = g >> 1, g1 = g & 1, c7 = c & 7, ch = c >> 3;
  const int wg = blockIdx.x, b = wg >> 5, h = wg & 31;
  unsigned stampv = 0; (void)stampv;
  const rsrc_t ws = mk_rsrc(a.ws, WS_END);
  u32x2 ep;

  if (wave == 0) STAMP(0);
  const u64 t_entry = now();
  const bool t_ffn = h < 14, t_q = h >= 16;
  const int u = wave - NS;
  const int tb = (b + TROT) & 7;
  const int tix = t_ffn ? h * 8 + tb : (h - 16) * 8 + tb;
  const int ld2 = (t_ffn ? F : H * DH) * 2;
  const int k0 = t_ffn ? 256 * (u & 3) : 512 * ((tix >> 6) & 1) + 128 * (u & 3);
  const rsrc_t rw1 = mk_rsrc((const char*)(t_ffn ? (tix < NFT ? a.wg : a.wu) : a.wq) + (size_t)k0 * ld2 + (t_ffn ? (tix < NFT ? tix : tix - NFT) : (tix & 63)) * 128,
                             (wave >= NS && (t_ffn | t_q) && !(ABL & 1)) ? 256 * ld2 : 0);
  const int voff1 = (32 * ch + 8 * g) * ld2 + c7 * 16;
  u32x4 R0[8];
  {
    const rsrc_t rx = mk_rsrc(a.x, B * D * 2);
    if (wave >= NS) {
#pragma unroll
      for (int i = 0; i < 8; ++i) R0[i] = ld16<AUX_NT>(rw1, voff1, i * ld2);
    } else {
#pragma unroll
      for (int i = 0; i < 4; ++i) R0[i] = ld16<0>(rx, wave * 2048 + lane * 16, (i & 1) * 1024 + (i >> 1) * 8192);
      R0[4] = R0[5] = R0[6] = R0[7] = u32x4{0u, 0u, 0u, 0u};
      const unsigned n = wave == 2 ? 16u : 0u;
      R0[5][0] = __builtin_amdgcn_raw_buffer_load_b32(mk_rsrc(a.kc + (size_t)wg * SK * DH, n), 0, 0, AUX_NT);
      R0[5][1] = __builtin_amdgcn_raw_buffer_load_b32(mk_rsrc(a.vc + (size_t)wg * SK * DH, n), 0, 0, AUX_NT);
      R0[5][2] = __builtin_amdgcn_raw_buffer_load_b32(mk_rsrc(a.wo + (size_t)h * 128 * D + b * 128, n), 0, 0, AUX_NT);
      R0[5][3] = __builtin_amdgcn_raw_buffer_load_b32(mk_rsrc(a.wd + (size_t)(h & 15) * 2 * FS * D + b * 128, n), 0, 0, AUX_NT);
      R0[6][0] = __builtin_amdgcn_raw_buffer_load_b32(mk_rsrc(a.ws + WS_Q + wg * 2048, n), 0, 0, AUX_NT);
      R0[6][1] = __builtin_amdgcn_raw_buffer_load_b32(mk_rsrc(a.ws + WS_P + wg * 8192, n), 0, 0, AUX_NT);
      R0[6][2] = __builtin_amdgcn_raw_buffer_load_b32(mk_rsrc(a.out, n), 0, 0, AUX_NT);
    }
    ep = __builtin_amdgcn_raw_buffer_load_b64(ws, WS_EPOCH, 0, 0);
    FENCE();
    if (wave == 0) { if (lane < 8) lds[L_FX + lane] = 0u; LDS_DONE(); }
    __builtin_amdgcn_s_barrier();
    FENCE();
  }

  if (wave < NS) {
    const rsrc_t rk = mk_rsrc(a.kc + (size_t)wg * SK * DH, SK * DH * 2);
    const rsrc_t rv = mk_rsrc(a.vc + (size_t)wg * SK * DH, SK * DH * 2);
    const int kvoff = lane * 16 + wave * T * 1024;
    if (t_ffn | t_q) {
#pragma unroll
      for (int i = 0; i < 2; ++i) ln_row(R0[2 * i], R0[2 * i + 1], lds + L_XN + (wave + 4 * i) * XN_STRIDE, lane);
      LDS_DONE();
      if (lane == 0) __hip_atomic_fetch_add(lds + L_FX, 1u, RLX_WG);
      FENCE();
    }
    asm volatile("" :: "v"(R0[4]), "v"(R0[5]), "v"(R0[6]));
    if ((t_ffn | t_q) && !(ABL & 1)) {
      const u64 limit = t_q ? SPIN_LIMIT : KV_T0;
      while (__hip_atomic_load(lds + L_FP, RLX_WG) != 4u && now() - t_entry < limit) __builtin_amdgcn_s_sleep(2);
      FENCE();
    }
    u32x4 Kr[T], Vr[T], X[NX + 1][2 * T];
#pragma unroll
    for (int i = 0; i < 2 * T; ++i) {
      if (i >= WIN_K) vm_wait<(WIN_K > 0 ? WIN_K - 1 : 0)>();
      (i < T ? Kr[i] : Vr[i - T]) = ld16<AUX_NT>(i < T ? rk : rv, kvoff + (i % T) * 1024, 0);
      FENCE();
    }
    int nx = 0;
    {
      const Tag tag = mk_tag(epoch_hash(ep));
      u32x4 v[2];
      gather_init(v, wave == 0, tag);
      auto qoff = [](int i) { return i * 1024; };
      const int qo = WS_Q + wg * 2048 + lane * 16;
      bool have = ABL & 1;
#pragma unroll
      for (int j = 0; j < NX; ++j) {
        if (!have) {
#pragma unroll
          for (int i = 0; i < 2 * T; ++i) {
            vm_wait<WIN_K - 1>();
            X[j][i] = ld16<AUX_NT>(i < T ? rk : rv, kvoff + (i % T) * 1024, (j + 1) * NS * T * 1024);
            FENCE();
          }
          nx = j + 1;
          have = wave == 0 ? gather_pass(v, ws, qo, tag, qoff) : __hip_atomic_load(lds + L_FQ, RLX_WG) == 1u;
        }
      }
      if (ABL & 1) {
      } else if (wave == 0) {
        if (!have) gather_wait(v, ws, qo, tag, qoff);
        lds[L_Q + lane] = pk_bf16(__uint_as_float(v[0][0]) + __uint_as_float(v[1][0]), __uint_as_float(v[0][2]) + __uint_as_float(v[1][2]));
        LDS_DONE();
        if (lane == 0) __hip_atomic_store(lds + L_FQ, 1u, RLX_WG);
        FENCE();
        STAMP(3);
      } else {
        lds_wait(lds + L_FQ, 1u);
      }
    }
    const u32x4 qv = *(LAS u32x4*)(lds + L_Q + c * 4);
    constexpr float SCALE = 0.08838834764831845f, LOG2E = 1.4426950408889634f;
    float m = -INFINITY, l = 0.f, acc[8];
#pragma unroll
    for (int k = 0; k < 8; ++k) acc[k] = 0.f;
    auto step = [&](u32x4* Kt, u32x4* Vt, const bool more, const int so) __attribute__((always_inline)) {
      float s[T];
#pragma unroll
      for (int i = 0; i < T; ++i) {
        float d = 0.f;
#pragma unroll
        for (int k = 0; k < 4; ++k) d = dot2(Kt[i][k], qv[k], d);
        asm volatile("" : "+v"(d));
        s[i] = d;
        SCHED_FENCE();
        if (more) { if (WIN_KK(i) < 2 * T) vm_wait_n(WIN_KK(i) - 1); Kt[i] = ld16<AUX_NT>(rk, kvoff + i * 1024, so); FENCE(); }
        SCHED_FENCE();
      }
      float mx = m;
#pragma unroll
      for (int i = 0; i < T; ++i) {
        s[i] = rbf(rbf(row16_sum(s[i])) * SCALE) * LOG2E;
        mx = fmaxf(mx, s[i]);
      }
      const float alpha = __builtin_amdgcn_exp2f(m - mx);
      m = mx; l *= alpha;
#pragma unroll
      for (int k = 0; k < 8; ++k) acc[k] *= alpha;
#pragma unroll
      for (int i = 0; i < T; ++i) { s[i] = __builtin_amdgcn_exp2f(s[i] - mx); l += s[i]; }
      SCHED_FENCE();
#pragma unroll
      for (int i = 0; i < T; ++i) {
#pragma unroll
        for (int k = 0; k < 4; ++k) { acc[2 * k] = fmaf(s[i], bf_lo(Vt[i][k]), acc[2 * k]); acc[2 * k + 1] = fmaf(s[i], bf_hi(Vt[i][k]), acc[2 * k + 1]); }
#pragma unroll
        for (int k = 0; k < 8; k += 2) { f32x2 p2 = {acc[k], acc[k + 1]}; asm volatile("" : "+v"(p2)); acc[k] = p2[0]; acc[k + 1] = p2[1]; }
        SCHED_FENCE();
        if (more) { if (WIN_VV(i) < 2 * T) vm_wait_n(WIN_VV(i) - 1); Vt[i] = ld16<AUX_NT>(rv, kvoff + i * 1024, so); FENCE(); }
        SCHED_FENCE();
      }
    };
    step(Kr, Vr, true, (nx + 1) * NS * T * 1024);
#pragma unroll
    for (int j = 0; j < NX; ++j)
      if (j < nx) step(&X[j][0], &X[j][T], false, 0);
    for (int tt = nx + 2; tt < NT; ++tt) step(Kr, Vr, true, tt * NS * T * 1024);
    step(Kr, Vr, false, 0);
    STAMP(4 + wave);
    const float M = xrow_max(m), f = __builtin_amdgcn_exp2f(m - M);
    l = xrow_sum(l * f);
#pragma unroll
    for (int k = 0; k < 8; ++k) acc[k] = xrow_sum(acc[k] * f);
    LAS unsigned* rec = lds + L_REC + wave * REC_STRIDE;
    if (g == 0) {
      *(LAS f32x4*)(rec + 8 * c) = f32x4{acc[0], acc[1], acc[2], acc[3]};
      *(LAS f32x4*)(rec + 8 * c + 4) = f32x4{acc[4], acc[5], acc[6], acc[7]};
      if (c == 0) *(LAS f32x2*)(rec + 128) = f32x2{M, l};
    }
    LDS_DONE();
    if (lane == 0) __hip_atomic_fetch_add(lds + L_FS, 1u, RLX_WG);
    STAMP_FLUSH();
    return;
  }

  if (ABL & 1) return;
  if (t_q) {
    u32x4 R[16];
#pragma unroll
    for (int i = 0; i < 8; ++i) { R[i] = R0[i]; vm_wait<WIN_Q - 1>(); R[8 + i] = ld16<AUX_NT>(rw1, voff1, (64 + i) * ld2); FENCE(); }
    lds_wait(lds + L_FX, (unsigned)NS);
    u32x4 A[2]; f32x4 acc[8];
#pragma unroll
    for (int ks = 0; ks < 2; ++ks) A[ks] = *(LAS u32x4*)(lds + L_XN + c7 * XN_STRIDE + (k0 >> 1) + ks * 32 + ch * 16 + g * 4);
    tile_tmfma(acc, A, R);
    if (tile64_partial(acc, lds + L_P1 + u * 512, lds + L_FP, lane)) {
      const Tag tag = mk_tag(epoch_hash(ep));
#pragma unroll
      for (int i = 0; i < 4; ++i) {
        const f32x2 s = tile64_sum(lds + L_P1, i, lane);
        st16_sc1(piece(s[0], s[1], tag), ws,
                 WS_Q + (gh * 32 + ((tix & 63) >> 1)) * 2048 + (tix >> 6) * 1024 + (tix & 1) * 512 + (lane & 31) * 16, i * 2 * 32 * 2048);
      }
      FENCE();
      STAMP(9);
    }
  } else if (t_ffn) {
    u32x4 R[32];
#pragma unroll
    for (int i = 0; i < 8; ++i) R[i] = R0[i];
#pragma unroll
    for (int i = 8; i < 32; ++i) { vm_wait<WIN_F - 1>(); R[i] = ld16<AUX_NT>(rw1, voff1, ((i >> 3) * 64 + (i & 7)) * ld2); FENCE(); }
    if (wave == 4) STAMP(34);
    lds_wait(lds + L_FX, (unsigned)NS);
    u32x4 A[4]; f32x4 acc[8];
#pragma unroll
    for (int ks = 0; ks < 4; ++ks) A[ks] = *(LAS u32x4*)(lds + L_XN + c7 * XN_STRIDE + (k0 >> 1) + ks * 32 + ch * 16 + g * 4);
    tile_tmfma(acc, A, R);
    if (tile64_partial(acc, lds + L_P1 + u * 512, lds + L_FP, lane)) {
      const Tag tag = mk_tag(epoch_hash(ep));
#pragma unroll
      for (int i = 0; i < 2; ++i) {
        f32x4 s = {0.f, 0.f, 0.f, 0.f};
#pragma unroll
        for (int w = 0; w < 4; ++w) s += *(LAS f32x4*)(lds + L_P1 + w * 512 + i * 256 + lane * 4);
        st16_sc1(piece(pk_bf16(s[0], s[1]), pk_bf16(s[2], s[3]), tag), ws,
                 (tix < NFT ? WS_G + tix * 256 : WS_U + (tix - NFT) * 256) + (g * F + c * 4) * 4, i * 4 * F * 4);
      }
      FENCE();
      STAMP(10);
    }
  }
  if (wave == 7 || (wave == 6 && !t_q)) { STAMP_FLUSH(); return; }

  u32x4 W[16];
  f32x4 acc[4];
  const int voff = (8 * g * D + 8 * c) * 2;
  u64 z;
  if (wave >= 5) {
    f32x4 a8[8];
    z = 0;
    if (t_q) {
      const int f0 = (2 * (h - 16) + (wave - 5)) * FS;
      LAS unsigned* ai = lds + L_AI + (wave - 5) * 8 * AI_STRIDE;
      u32x4 R[32];
      const rsrc_t rw = mk_rsrc(a.wd + (size_t)f0 * D + b * 128, FS * D * 2);
      tile_load<32, WIN_S>(R, rw, voff, g >= 2 ? 0x7ffffff0 : voff, 32 * D * 2, D * 2);
      if (wave == 5) STAMP(19);
      const Tag tag = mk_tag(epoch_hash(ep));
      u32x4 v[8];
      gather(v, ws, WS_G + (gh * F + f0 + (lane & 31) * 4) * 4, (lane & 31) < FS / 4, tag, [](int i) { return i * 2 * F * 4; });
      if (wave == 5) STAMP(21);
#pragma unroll
      for (int i = 0; i < 4; ++i) {
        const u32x4 gt = v[i], up = v[4 + i];
        *(LAS u32x2*)(ai + (2 * i + gh) * AI_STRIDE + (lane & 31) * 2) =
            u32x2{pk_bf16(bf_lo(gt[0]) * bf_lo(up[0]), bf_hi(gt[0]) * bf_hi(up[0])), pk_bf16(bf_lo(gt[2]) * bf_lo(up[2]), bf_hi(gt[2]) * bf_hi(up[2]))};
      }
      LDS_DONE();
      u32x4 A[4];
#pragma unroll
      for (int ks = 0; ks < 4; ++ks) A[ks] = *(LAS u32x4*)(ai + c7 * AI_STRIDE + ks * 16 + g * 4);
      tile_tmfma(a8, A, R);
      if (wave == 6) {
#pragma unroll
        for (int e = 0; e < 8; ++e) *(LAS f32x4*)(lds + L_A6 + e * 256 + lane * 4) = a8[e];
        LDS_DONE();
        if (lane == 0) __hip_atomic_store(lds + L_F6, 1u, RLX_WG);
        STAMP_FLUSH();
        return;
      }
      lds_wait(lds + L_F6, 1u);
#pragma unroll
      for (int e = 0; e < 8; ++e) a8[e] += *(LAS f32x4*)(lds + L_A6 + e * 256 + lane * 4);
    } else {
#pragma unroll
      for (int e = 0; e < 8; ++e) a8[e] = f32x4{0.f, 0.f, 0.f, 0.f};
    }
    z = epoch_hash(ep);
#pragma unroll
    for (int e = 0; e < 4; ++e) { *(LAS f32x4*)(lds + L_ACC + e * 256 + lane * 4) = a8[e]; acc[e] = a8[4 + e]; }
    LDS_DONE();
    if (lane == 0) __hip_atomic_store(lds + L_FA, 1u, RLX_WG);
    lds_wait(lds + L_FW, 1u);
#pragma unroll
    for (int i = 0; i < 16; ++i) { W[i] = *(LAS u32x4*)(lds + L_WO + i * 256 + lane * 4); asm volatile("" : "+v"(W[i])); }
    STAMP(22);
    __builtin_amdgcn_s_setprio(2);
    lds_wait(lds + L_FO, 1u);
  } else {
    {
      u32x4 R[32];
      tile_load<32, WIN_S>(R, mk_rsrc(a.wo + (size_t)h * 128 * D + b * 128, 128 * D * 2), voff, voff, 32 * D * 2, D * 2);
#pragma unroll
      for (int ks = 0; ks < 4; ++ks)
#pragma unroll
        for (int e = 0; e < 4; ++e) {
          W[ks * 4 + e] = tile_frag(R, ks, e);
          asm volatile("" : "+v"(W[ks * 4 + e]));
          *(LAS u32x4*)(lds + L_WO + (ks * 4 + e) * 256 + lane * 4) = tile_frag(R, ks, 4 + e);
        }
      LDS_DONE();
      if (lane == 0) __hip_atomic_store(lds + L_FW, 1u, RLX_WG);
    }
    z = epoch_hash(ep);
    const Tag tag = mk_tag(z);
    STAMP(13);
    lds_wait(lds + L_FA, 1u);
#pragma unroll
    for (int e = 0; e < 4; ++e) { acc[e] = *(LAS f32x4*)(lds + L_ACC + e * 256 + lane * 4); asm volatile("" : "+v"(acc[e])); }
    __builtin_amdgcn_s_setprio(2);
    lds_wait(lds + L_FS, (unsigned)NS);
    STAMP(14);
    float mw[NS], lw[NS]; f32x2 aw[NS];
#pragma unroll
    for (int w = 0; w < NS; ++w) {
      const f32x2 ml = *(LAS f32x2*)(lds + L_REC + w * REC_STRIDE + 128);
      mw[w] = ml[0]; lw[w] = ml[1];
      aw[w] = *(LAS f32x2*)(lds + L_REC + w * REC_STRIDE + 2 * lane);
    }
    float M = mw[0];
#pragma unroll
    for (int w = 1; w < NS; ++w) M = fmaxf(M, mw[w]);
    float L = 0.f, o0 = 0.f, o1 = 0.f;
#pragma unroll
    for (int w = 0; w < NS; ++w) { const float f = __builtin_amdgcn_exp2f(mw[w] - M); L = fmaf(f, lw[w], L); o0 = fmaf(f, aw[w][0], o0); o1 = fmaf(f, aw[w][1], o1); }
    const float inv = __builtin_amdgcn_rcpf(L);
    const unsigned op = pk_bf16(div_by(o0, L, inv), div_by(o1, L, inv)), on = dppu<XOR1>(op);
    if ((lane & 1) == 0) st16_sc1(piece(op, on, tag), ws, WS_O + (h * 8 + b) * 512 + lane * 8, 0);
    STAMP(15);
    u32x4 v[4];
    gather(v, ws, WS_O + h * 4096 + lane * 16, true, tag, [](int i) { return i * 1024; });
    STAMP(16);
#pragma unroll
    for (int i = 0; i < 4; ++i) *(LAS u32x2*)(lds + L_AI + (2 * i + gh) * AI_STRIDE + (lane & 31) * 2) = u32x2{v[i][0], v[i][2]};
    LDS_DONE();
    if (lane == 0) __hip_atomic_store(lds + L_FO, 1u, RLX_WG);
    FENCE();
  }
  const Tag tag = mk_tag(z);
#pragma unroll
  for (int ks = 0; ks < 4; ++ks) {
    const u32x4 A = *(LAS u32x4*)(lds + L_AI + c7 * AI_STRIDE + ks * 16 + g * 4);
#pragma unroll
    for (int e = 0; e < 4; ++e) acc[e] = mfma(A, W[ks * 4 + e], acc[e]);
  }
#pragma unroll
  for (int r = 0; r < 4; ++r) {
    const float v0 = gh ? acc[2][r] : acc[0][r], v1 = gh ? acc[3][r] : acc[1][r];
    st16_sc1(piece(v0, v1, tag), ws,
             WS_P + ((b * 32 + 4 * g1 * 4 + (c >> 2)) * 32 + h) * 256 + u * 128 + ((c & 3) * 2 + gh) * 16, r * 4 * 32 * 256);
  }
  if (wave == 5) { STAMP_FLUSH(); return; }
  STAMP(17);
  {
    const int col = b * 128 + (h & 3) * 32 + (c7 >> 1) * 8 + ch * 4 + (c & 1) * 2;
    const unsigned xres = *(const GAS unsigned*)(a.x + (h >> 2) * D + col);
    u32x4 p[8];
    gather(p, ws, WS_P + (b * 32 + h) * 8192 + lane * 16, true, tag, [](int i) { return i * 1024; });
    float y0 = 0.f, y1 = 0.f;
#pragma unroll
    for (int i = 0; i < 8; ++i) { y0 += __uint_as_float(p[i][0]); y1 += __uint_as_float(p[i][2]); }
    y0 = xrow_sum(y0); y1 = xrow_sum(y1);
    if (g == 0) *(GAS unsigned*)(a.out + (h >> 2) * D + col) = pk_bf16(bf_lo(xres) + y0, bf_hi(xres) + y1);
  }
  if (wg == 0 && lane == 0) st8_sc1(u32x2{(unsigned)z, (unsigned)(z >> 32)}, ws, WS_EPOCH, 0);
  STAMP(18);
  STAMP_FLUSH();
}

}

extern "C" void kernel_launch(void* const* d_in, const int* in_sizes, int n_in, void* d_out, int out_size, void* d_ws, size_t ws_size,
                              hipStream_t stream) {
  Args a;
  a.x = (const unsigned short*)d_in[0];
  a.kc = (const unsigned short*)d_in[1];
  a.vc = (const unsigned short*)d_in[2];
  a.wq = (const unsigned short*)d_in[3];
  a.wo = (const unsigned short*)d_in[4];
  a.wg = (const unsigned short*)d_in[6];
  a.wu = (const unsigned short*)d_in[7];
  a.wd = (const unsigned short*)d_in[8];
  a.out = (unsigned short*)d_out;
  a.ws = (unsigned char*)d_ws;
  fused_decode<<<B * H, 512, 0, stream>>>(a);
}
```

```cpp
#include <hip/hip_runtime.h>
#include <hip/hip_bf16.h>
#include <stdint.h>

#define STAMPS 0
#define KV_T 4
#define POLL_SLEEP 2
#define WIN_Q 8
#define WIN_F 8
#define WIN_S 6
#define PRE 0
#define NX 0
#define WIN_K 8
#define WIN_KV 5
#define ABL 0
#define TROT 0
#define KV_T0 420
#define WMASK 0
#define WIN_KK(i) (WIN_KV + ((WMASK >> (i)) & 1))
#define WIN_VV(i) (WIN_KV + ((WMASK >> (4 + (i))) & 1))
#define TOUCH 0
#define SKEW 0
#define CODE_PF 0

namespace {

typedef unsigned u32x4 __attribute__((ext_vector_type(4)));
typedef unsigned u32x2 __attribute__((ext_vector_type(2)));
typedef float f32x4 __attribute__((ext_vector_type(4)));
typedef float f32x2 __attribute__((ext_vector_type(2)));
typedef __bf16 bf16x2 __attribute__((ext_vector_type(2)));
typedef __bf16 bf16x8 __attribute__((ext_vector_type(8)));
typedef unsigned long long u64;
#define GAS __attribute__((address_space(1)))
#define LAS __attribute__((address_space(3)))
#define RLX_WG __ATOMIC_RELAXED, __HIP_MEMORY_SCOPE_WORKGROUP
typedef __amdgpu_buffer_rsrc_t rsrc_t;

constexpr int B = 8, D = 1024, H = 32, SK = 4096, DH = 128, F = 3584;
constexpr int FS = F / 32;
constexpr int NQT = H * DH / 64, NFT = F / 64;
constexpr int AUX_NT = 2, AUX_SC1 = 16;
constexpr int NS = 4, T = KV_T;
constexpr int NT = (SK / 4) / (NS * T);

constexpr int WS_EPOCH = 0;
constexpr int WS_Q = 4096;
constexpr int WS_G = WS_Q + B * H * 2 * DH * 8;
constexpr int WS_U = WS_G + B * F * 4;
constexpr int WS_O = WS_U + B * F * 4;
constexpr int WS_P = WS_O + B * H * DH * 4;
constexpr int WS_END = WS_P + 256 * 8192;
constexpr int WS_STAMP = 16 << 20;

constexpr u64 SPIN_LIMIT = 2000000;

constexpr int XN_STRIDE = 516;
constexpr int L_XN = 0;
constexpr int L_P1 = L_XN + 8 * XN_STRIDE;
constexpr int L_A6 = L_P1 + 2048;
constexpr int L_ACC = L_A6 + 2048;
constexpr int L_WO = L_ACC + 1024;
constexpr int AI_STRIDE = 68;
constexpr int L_AI = L_WO + 4096;
constexpr int L_Q = L_AI + 16 * AI_STRIDE;
constexpr int L_REC = L_Q + 64;
constexpr int REC_STRIDE = 132;
constexpr int L_FX = L_REC + NS * REC_STRIDE;
constexpr int L_FP = L_FX + 1, L_FQ = L_FX + 2, L_FS = L_FX + 3, L_FA = L_FX + 4, L_FW = L_FX + 5, L_FO = L_FX + 6, L_F6 = L_FX + 7;
constexpr int L_END = L_FX + 8;

__device__ __forceinline__ float bf_lo(unsigned u) { return __uint_as_float(u << 16); }
__device__ __forceinline__ float bf_hi(unsigned u) { return __uint_as_float(u & 0xffff0000u); }
__device__ __forceinline__ unsigned pk_bf16(float lo, float hi) {
  const f32x2 v = {lo, hi};
  return __builtin_bit_cast(unsigned, __builtin_convertvector(v, bf16x2));
}
__device__ __forceinline__ float rbf(float x) { return bf_lo(pk_bf16(x, x)); }
__device__ __forceinline__ float dot2(unsigned k, unsigned q, float acc) {
  return __builtin_amdgcn_fdot2_f32_bf16(__builtin_bit_cast(bf16x2, k), __builtin_bit_cast(bf16x2, q), acc, false);
}
template <int CTRL> __device__ __forceinline__ unsigned dppu(unsigned x) { return (unsigned)__builtin_amdgcn_mov_dpp((int)x, CTRL, 0xf, 0xf, true); }
template <int CTRL> __device__ __forceinline__ float dpp(float x) { return __uint_as_float(dppu<CTRL>(__float_as_uint(x))); }
constexpr int XOR1 = 0xB1, XOR2 = 0x4E, XOR7 = 0x141, XOR8 = 0x128;
__device__ __forceinline__ float row16_sum(float d) {
  d += dpp<XOR1>(d); d += dpp<XOR2>(d); d += dpp<XOR7>(d); d += dpp<XOR8>(d);
  return d;
}
__device__ __forceinline__ float xrow_sum(float x) {
  auto s = __builtin_amdgcn_permlane16_swap(__float_as_uint(x), __float_as_uint(x), false, false);
  x = __uint_as_float(s[0]) + __uint_as_float(s[1]);
  auto t = __builtin_amdgcn_permlane32_swap(__float_as_uint(x), __float_as_uint(x), false, false);
  return __uint_as_float(t[0]) + __uint_as_float(t[1]);
}
__device__ __forceinline__ float xrow_max(float x) {
  auto s = __builtin_amdgcn_permlane16_swap(__float_as_uint(x), __float_as_uint(x), false, false);
  x = fmaxf(__uint_as_float(s[0]), __uint_as_float(s[1]));
  auto t = __builtin_amdgcn_permlane32_swap(__float_as_uint(x), __float_as_uint(x), false, false);
  return fmaxf(__uint_as_float(t[0]), __uint_as_float(t[1]));
}
__device__ __forceinline__ rsrc_t mk_rsrc(const void* p, unsigned bytes) {
  return __builtin_amdgcn_make_buffer_rsrc(const_cast<void*>(p), 0, bytes, 0x00020000);
}
template <int AUX> __device__ __forceinline__ u32x4 ld16(rsrc_t r, int voff, int soff) { return __builtin_amdgcn_raw_buffer_load_b128(r, voff, soff, AUX); }
__device__ __forceinline__ void st16_sc1(u32x4 v, rsrc_t r, int voff, int off) { __builtin_amdgcn_raw_buffer_store_b128(v, r, voff + off, 0, AUX_SC1); }
__device__ __forceinline__ void st8_sc1(u32x2 v, rsrc_t r, int voff, int soff) { __builtin_amdgcn_raw_buffer_store_b64(v, r, voff, soff, AUX_SC1); }
#define FENCE() asm volatile("" ::: "memory")
#define LDS_DONE() asm volatile("s_waitcnt lgkmcnt(0)" ::: "memory")
#define SCHED_FENCE() __builtin_amdgcn_sched_barrier(0)
template <int N> __device__ __forceinline__ void vm_wait() { asm volatile("s_waitcnt vmcnt(%0)" ::"n"(N) : "memory"); }
__device__ __forceinline__ void vm_wait_n(int n) {
  switch (n) { case 3: vm_wait<3>(); break; case 4: vm_wait<4>(); break; case 5: vm_wait<5>(); break; case 6: vm_wait<6>(); break; default: vm_wait<7>(); }
}
__device__ __forceinline__ u64 now() { return __builtin_amdgcn_s_memrealtime(); }

struct Tag { unsigned a, b; };
__device__ __forceinline__ bool fresh(const u32x4& v, const Tag& t) { return (v[1] == t.a) & (v[3] == t.b); }
__device__ __forceinline__ u32x4 piece(unsigned p0, unsigned p1, const Tag& t) { return u32x4{p0, t.a, p1, t.b}; }
__device__ __forceinline__ u32x4 piece(float p0, float p1, const Tag& t) { return u32x4{__float_as_uint(p0), t.a, __float_as_uint(p1), t.b}; }

template <int N> __device__ __forceinline__ void gather_init(u32x4 (&v)[N], bool act, const Tag& t) {
#pragma unroll
  for (int i = 0; i < N; ++i) v[i] = u32x4{0u, act ? ~t.a : t.a, 0u, t.b};
}
template <int N, class Off> __device__ __forceinline__ bool gather_pass(u32x4 (&v)[N], rsrc_t ws, int voff, const Tag& t, Off off) {
  FENCE();
#pragma unroll
  for (int i = 0; i < N; ++i)
    if (!fresh(v[i], t)) v[i] = ld16<AUX_SC1>(ws, voff, off(i));
  bool ok = true;
#pragma unroll
  for (int i = 0; i < N; ++i) ok &= fresh(v[i], t);
  const bool all = __all(ok);
  FENCE();
  return all;
}
template <int N, class Off> __device__ __forceinline__ void gather_wait(u32x4 (&v)[N], rsrc_t ws, int voff, const Tag& t, Off off) {
  const u64 t0 = now();
  while (!gather_pass(v, ws, voff, t, off)) {
    if (now() - t0 > SPIN_LIMIT) break;
    __builtin_amdgcn_s_sleep(POLL_SLEEP);
  }
}
template <int N, class Off> __device__ __forceinline__ void gather(u32x4 (&v)[N], rsrc_t ws, int voff, bool act, const Tag& t, Off off) {
  gather_init(v, act, t);
  gather_wait(v, ws, voff, t, off);
}
__device__ __forceinline__ u64 epoch_hash(u32x2 e) {
  asm volatile("" : "+v"(e));
  u64 z = (((u64)(unsigned)__builtin_amdgcn_readfirstlane(e[1]) << 32) | (unsigned)__builtin_amdgcn_readfirstlane(e[0])) + 0x9E3779B97F4A7C15ull;
  z = (z ^ (z >> 30)) * 0xBF58476D1CE4E5B9ull; z = (z ^ (z >> 27)) * 0x94D049BB133111EBull;
  return z ^ (z >> 31);
}
__device__ __forceinline__ Tag mk_tag(u64 z) { return Tag{(unsigned)z, (unsigned)(z >> 32)}; }
__device__ __forceinline__ void lds_wait(LAS unsigned* w, unsigned want) {
  const u64 t0 = now();
  while (__hip_atomic_load(w, RLX_WG) != want) {
    if (now() - t0 > SPIN_LIMIT) break;
    __builtin_amdgcn_s_sleep(1);
  }
  FENCE();
}

template <int N, int WIN> __device__ __forceinline__ void tile_load(u32x4 (&R)[N], rsrc_t r, int voff, int voff3, int step, int row) {
#pragma unroll
  for (int i = 0; i < N; ++i) {
    if (i >= WIN) vm_wait<WIN - 1>();
    R[i] = ld16<AUX_NT>(r, i < 24 ? voff : voff3, (i >> 3) * step + (i & 7) * row);
    FENCE();
  }
}
template <int N> __device__ __forceinline__ u32x4 tile_frag(const u32x4 (&R)[N], int ks, int e) {
  u32x4 f;
#pragma unroll
  for (int p = 0; p < 4; ++p)
    f[p] = __builtin_amdgcn_perm(R[ks * 8 + 2 * p + 1][e >> 1], R[ks * 8 + 2 * p][e >> 1], (e & 1) ? 0x07060302u : 0x05040100u);
  return f;
}
__device__ __forceinline__ f32x4 mfma(const u32x4& a, const u32x4& b, const f32x4& c) {
  return __builtin_amdgcn_mfma_f32_16x16x32_bf16(__builtin_bit_cast(bf16x8, a), __builtin_bit_cast(bf16x8, b), c, 0, 0, 0);
}
template <int N> __device__ __forceinline__ void tile_tmfma(f32x4 (&acc)[8], const u32x4 (&A)[N / 8], const u32x4 (&R)[N]) {
#pragma unroll
  for (int e = 0; e < 8; ++e) acc[e] = f32x4{0.f, 0.f, 0.f, 0.f};
#pragma unroll
  for (int ks = 0; ks < N / 8; ++ks) {
#pragma unroll
    for (int e = 0; e < 8; ++e) acc[e] = mfma(A[ks], tile_frag(R, ks, e), acc[e]);
    SCHED_FENCE();
  }
}
__device__ __forceinline__ bool tile64_partial(const f32x4 (&acc)[8], LAS unsigned* part, LAS unsigned* counter, int lane) {
  const int gh = lane >> 5, g1 = (lane >> 4) & 1, ch = (lane >> 3) & 1, c7 = lane & 7;
  float y[2][8];
#pragma unroll
  for (int e = 0; e < 8; ++e)
#pragma unroll
    for (int r = 0; r < 2; ++r) {
      auto s = __builtin_amdgcn_permlane32_swap(__float_as_uint(acc[e][r]), __float_as_uint(acc[e][r + 2]), false, false);
      const float own = gh ? __uint_as_float(s[1]) : __uint_as_float(s[0]), other = gh ? __uint_as_float(s[0]) : __uint_as_float(s[1]);
      y[r][e] = own + dpp<XOR8>(other);
    }
  if (gh == ch) {
#pragma unroll
    for (int r = 0; r < 2; ++r) {
      LAS unsigned* d = part + (4 * g1 + 2 * gh + r) * 64 + c7 * 8;
      *(LAS f32x4*)d = f32x4{y[r][0], y[r][1], y[r][2], y[r][3]};
      *(LAS f32x4*)(d + 4) = f32x4{y[r][4], y[r][5], y[r][6], y[r][7]};
    }
  }
  LDS_DONE();
  unsigned arrived = 0;
  if (lane == 0) arrived = __hip_atomic_fetch_add(counter, 1u, RLX_WG);
  const bool last = __builtin_amdgcn_readfirstlane(arrived) == 3u;
  FENCE();
  return last;
}
__device__ __forceinline__ f32x2 tile64_sum(LAS unsigned* part, int i, int lane) {
  f32x2 s = {0.f, 0.f};
#pragma unroll
  for (int w = 0; w < 4; ++w) s += *(LAS f32x2*)(part + w * 512 + i * 128 + lane * 2);
  return s;
}

struct Args {
  const unsigned short *x, *kc, *vc, *wq, *wo, *wg, *wu, *wd;
  unsigned short* out;
  unsigned char* ws;
};

#define STAMP(i) do {} while (0)
#define STAMP_FLUSH() do {} while (0)

__device__ __forceinline__ float div_by(float a, float d, float rd) {
  const float q = a * rd;
  return fmaf(fmaf(-q, d, a), rd, q);
}
__device__ __forceinline__ void ln_row(const u32x4& x0, const u32x4& x1, LAS unsigned* dst, int lane) {
  float v[16];
#pragma unroll
  for (int k = 0; k < 4; ++k) { v[2 * k] = bf_lo(x0[k]); v[2 * k + 1] = bf_hi(x0[k]); v[8 + 2 * k] = bf_lo(x1[k]); v[8 + 2 * k + 1] = bf_hi(x1[k]); }
  float s1 = 0.f;
#pragma unroll
  for (int k = 0; k < 8; ++k) s1 += v[k] + v[8 + k];
  const float mean = xrow_sum(row16_sum(s1)) * (1.f / D);
  float s2 = 0.f;
#pragma unroll
  for (int k = 0; k < 16; ++k) s2 = fmaf(v[k] - mean, v[k] - mean, s2);
  const float var = xrow_sum(row16_sum(s2)) * (1.f / D);
  const float m = rbf(mean), den = rbf(sqrtf(rbf(rbf(var) + 1e-5f))), rden = __builtin_amdgcn_rcpf(den);
  u32x4 o0, o1;
#pragma unroll
  for (int k = 0; k < 4; ++k) {
    const unsigned d0 = pk_bf16(v[2 * k] - m, v[2 * k + 1] - m), d1 = pk_bf16(v[8 + 2 * k] - m, v[8 + 2 * k + 1] - m);
    o0[k] = pk_bf16(div_by(bf_lo(d0), den, rden), div_by(bf_hi(d0), den, rden));
    o1[k] = pk_bf16(div_by(bf_lo(d1), den, rden), div_by(bf_hi(d1), den, rden));
  }
  *(LAS u32x4*)(dst + lane * 4) = o0;
  *(LAS u32x4*)(dst + 256 + lane * 4) = o1;
}

__global__ void __launch_bounds__(512, 2) fused_decode(Args a) {
  __shared__ __attribute__((aligned(16))) unsigned lds_[L_END];
  LAS unsigned* lds = (LAS unsigned*)lds_;
  asm volatile("" :: "s"(a.x), "s"(a.kc), "s"(a.vc), "s"(a.wq), "s"(a.wo), "s"(a.wg), "s"(a.wu), "s"(a.wd), "s"(a.out), "s"(a.ws));
  const int lane = threadIdx.x & 63, wave = __builtin_amdgcn_readfirstlane(threadIdx.x >> 6);
  const int g = lane >> 4, c = lane & 15, gh = g >> 1, g1 = g & 1, c7 = c & 7, ch = c >> 3;
  const int wg = blockIdx.x, b = wg >> 5, h = wg & 31;
  unsigned stampv = 0; (void)stampv;
  const rsrc_t ws = mk_rsrc(a.ws, WS_END);
  u32x2 ep;

  if (wave == 0) STAMP(0);
  const u64 t_entry = now();
  const bool t_ffn = h < 14, t_q = h >= 16;
  const int u = wave - NS;
  const int tb = (b + TROT) & 7;
  const int tix = t_ffn ? h * 8 + tb : (h - 16) * 8 + tb;
  const int ld2 = (t_ffn ? F : H * DH) * 2;
  const int k0 = t_ffn ? 256 * (u & 3) : 512 * ((tix >> 6) & 1) + 128 * (u & 3);
  const rsrc_t rw1 = mk_rsrc((const char*)(t_ffn ? (tix < NFT ? a.wg : a.wu) : a.wq) + (size_t)k0 * ld2 + (t_ffn ? (tix < NFT ? tix : tix - NFT) : (tix & 63)) * 128,
                             (wave >= NS && (t_ffn | t_q) && !(ABL & 1)) ? 256 * ld2 : 0);
  const int voff1 = (32 * ch + 8 * g) * ld2 + c7 * 16;
  u32x4 R0[8];
  {
    const rsrc_t rx = mk_rsrc(a.x, B * D * 2);
    if (wave >= NS) {
#pragma unroll
      for (int i = 0; i < 8; ++i) R0[i] = ld16<AUX_NT>(rw1, voff1, i * ld2);
    } else {
#pragma unroll
      for (int i = 0; i < 4; ++i) R0[i] = ld16<0>(rx, wave * 2048 + lane * 16, (i & 1) * 1024 + (i >> 1) * 8192);
      R0[4] = R0[5] = R0[6] = R0[7] = u32x4{0u, 0u, 0u, 0u};
    }
    ep = __builtin_amdgcn_raw_buffer_load_b64(ws, WS_EPOCH, 0, 0);
    FENCE();
    if (wave == 0) { if (lane < 8) lds[L_FX + lane] = 0u; LDS_DONE(); }
    __builtin_amdgcn_s_barrier();
    FENCE();
  }

  if (wave < NS) {
    const rsrc_t rk = mk_rsrc(a.kc + (size_t)wg * SK * DH, SK * DH * 2);
    const rsrc_t rv = mk_rsrc(a.vc + (size_t)wg * SK * DH, SK * DH * 2);
    const int kvoff = lane * 16 + wave * T * 1024;
    const int rot = (SKEW) & (NT - 1);
    auto toff = [&](int tt) { return ((tt + rot) & (NT - 1)) * (NS * T * 1024); };
    if (t_ffn | t_q) {
#pragma unroll
      for (int i = 0; i < 2; ++i) ln_row(R0[2 * i], R0[2 * i + 1], lds + L_XN + (wave + 4 * i) * XN_STRIDE, lane);
      LDS_DONE();
      if (lane == 0) __hip_atomic_fetch_add(lds + L_FX, 1u, RLX_WG);
      FENCE();
    }
    if ((t_ffn | t_q) && !(ABL & 1)) {
      const u64 limit = t_q ? SPIN_LIMIT : KV_T0;
      while (__hip_atomic_load(lds + L_FP, RLX_WG) != 4u && now() - t_entry < limit) __builtin_amdgcn_s_sleep(2);
      FENCE();
    }
    u32x4 Kr[T], Vr[T], X[NX + 1][2 * T];
#pragma unroll
    for (int i = 0; i < 2 * T; ++i) {
      if (i >= WIN_K) vm_wait<(WIN_K > 0 ? WIN_K - 1 : 0)>();
      (i < T ? Kr[i] : Vr[i - T]) = ld16<AUX_NT>(i < T ? rk : rv, kvoff + (i % T) * 1024, toff(0));
      FENCE();
    }
    int nx = 0;
    {
      const Tag tag = mk_tag(epoch_hash(ep));
      u32x4 v[2];
      gather_init(v, wave == 0, tag);
      auto qoff = [](int i) { return i * 1024; };
      const int qo = WS_Q + wg * 2048 + lane * 16;
      bool have = ABL & 1;
#pragma unroll
      for (int j = 0; j < NX; ++j) {
        if (!have) {
#pragma unroll
          for (int i = 0; i < 2 * T; ++i) {
            vm_wait<WIN_K - 1>();
            X[j][i] = ld16<AUX_NT>(i < T ? rk : rv, kvoff + (i % T) * 1024, toff(j + 1));
            FENCE();
          }
          nx = j + 1;
          have = wave == 0 ? gather_pass(v, ws, qo, tag, qoff) : __hip_atomic_load(lds + L_FQ, RLX_WG) == 1u;
        }
      }
      if (ABL & 1) {
      } else if (wave == 0) {
        if (!have) gather_wait(v, ws, qo, tag, qoff);
        lds[L_Q + lane] = pk_bf16(__uint_as_float(v[0][0]) + __uint_as_float(v[1][0]), __uint_as_float(v[0][2]) + __uint_as_float(v[1][2]));
        LDS_DONE();
        if (lane == 0) __hip_atomic_store(lds + L_FQ, 1u, RLX_WG);
        FENCE();
        STAMP(3);
      } else {
        lds_wait(lds + L_FQ, 1u);
      }
    }
    const u32x4 qv = *(LAS u32x4*)(lds + L_Q + c * 4);
    constexpr float SCALE = 0.08838834764831845f, LOG2E = 1.4426950408889634f;
    float m = -INFINITY, l = 0.f, acc[8];
#pragma unroll
    for (int k = 0; k < 8; ++k) acc[k] = 0.f;
    auto step = [&](u32x4* Kt, u32x4* Vt, const bool more, const int so) __attribute__((always_inline)) {
      float s[T];
#pragma unroll
      for (int i = 0; i < T; ++i) {
        float d = 0.f;
#pragma unroll
        for (int k = 0; k < 4; ++k) d = dot2(Kt[i][k], qv[k], d);
        asm volatile("" : "+v"(d));
        s[i] = d;
        SCHED_FENCE();
        if (more) { if (WIN_KK(i) < 2 * T) vm_wait_n(WIN_KK(i) - 1); Kt[i] = ld16<AUX_NT>(rk, kvoff + i * 1024, so); FENCE(); }
        SCHED_FENCE();
      }
      float mx = m;
#pragma unroll
      for (int i = 0; i < T; ++i) {
        s[i] = rbf(rbf(row16_sum(s[i])) * SCALE) * LOG2E;
        mx = fmaxf(mx, s[i]);
      }
      const float alpha = __builtin_amdgcn_exp2f(m - mx);
      m = mx; l *= alpha;
#pragma unroll
      for (int k = 0; k < 8; ++k) acc[k] *= alpha;
#pragma unroll
      for (int i = 0; i < T; ++i) { s[i] = __builtin_amdgcn_exp2f(s[i] - mx); l += s[i]; }
      SCHED_FENCE();
#pragma unroll
      for (int i = 0; i < T; ++i) {
#pragma unroll
        for (int k = 0; k < 4; ++k) { acc[2 * k] = fmaf(s[i], bf_lo(Vt[i][k]), acc[2 * k]); acc[2 * k + 1] = fmaf(s[i], bf_hi(Vt[i][k]), acc[2 * k + 1]); }
#pragma unroll
        for (int k = 0; k < 8; k += 2) { f32x2 p2 = {acc[k], acc[k + 1]}; asm volatile("" : "+v"(p2)); acc[k] = p2[0]; acc[k + 1] = p2[1]; }
        SCHED_FENCE();
        if (more) { if (WIN_VV(i) < 2 * T) vm_wait_n(WIN_VV(i) - 1); Vt[i] = ld16<AUX_NT>(rv, kvoff + i * 1024, so); FENCE(); }
        SCHED_FENCE();
      }
    };
    step(Kr, Vr, true, toff(nx + 1));
#pragma unroll
    for (int j = 0; j < NX; ++j)
      if (j < nx) step(&X[j][0], &X[j][T], false, 0);
    for (int tt = nx + 2; tt < NT; ++tt) step(Kr, Vr, true, toff(tt));
    step(Kr, Vr, false, 0);
    STAMP(4 + wave);
    const float M = xrow_max(m), f = __builtin_amdgcn_exp2f(m - M);
    l = xrow_sum(l * f);
#pragma unroll
    for (int k = 0; k < 8; ++k) acc[k] = xrow_sum(acc[k] * f);
    LAS unsigned* rec = lds + L_REC + wave * REC_STRIDE;
    if (g == 0) {
      *(LAS f32x4*)(rec + 8 * c) = f32x4{acc[0], acc[1], acc[2], acc[3]};
      *(LAS f32x4*)(rec + 8 * c + 4) = f32x4{acc[4], acc[5], acc[6], acc[7]};
      if (c == 0) *(LAS f32x2*)(rec + 128) = f32x2{M, l};
    }
    LDS_DONE();
    if (lane == 0) __hip_atomic_fetch_add(lds + L_FS, 1u, RLX_WG);
    STAMP_FLUSH();
    return;
  }

  if (ABL & 1) return;
  if (t_q) {
    u32x4 R[16];
#pragma unroll
    for (int i = 0; i < 8; ++i) { R[i] = R0[i]; vm_wait<WIN_Q - 1>(); R[8 + i] = ld16<AUX_NT>(rw1, voff1, (64 + i) * ld2); FENCE(); }
    lds_wait(lds + L_FX, (unsigned)NS);
    u32x4 A[2]; f32x4 acc[8];
#pragma unroll
    for (int ks = 0; ks < 2; ++ks) A[ks] = *(LAS u32x4*)(lds + L_XN + c7 * XN_STRIDE + (k0 >> 1) + ks * 32 + ch * 16 + g * 4);
    tile_tmfma(acc, A, R);
    if (tile64_partial(acc, lds + L_P1 + u * 512, lds + L_FP, lane)) {
      const Tag tag = mk_tag(epoch_hash(ep));
#pragma unroll
      for (int i = 0; i < 4; ++i) {
        const f32x2 s = tile64_sum(lds + L_P1, i, lane);
        st16_sc1(piece(s[0], s[1], tag), ws,
                 WS_Q + (gh * 32 + ((tix & 63) >> 1)) * 2048 + (tix >> 6) * 1024 + (tix & 1) * 512 + (lane & 31) * 16, i * 2 * 32 * 2048);
      }
      FENCE();
      STAMP(9);
    }
  } else if (t_ffn) {
    u32x4 R[32];
#pragma unroll
    for (int i = 0; i < 8; ++i) R[i] = R0[i];
#pragma unroll
    for (int i = 8; i < 32; ++i) { vm_wait<WIN_F - 1>(); R[i] = ld16<AUX_NT>(rw1, voff1, ((i >> 3) * 64 + (i & 7)) * ld2); FENCE(); }
    if (wave == 4) STAMP(34);
    lds_wait(lds + L_FX, (unsigned)NS);
    u32x4 A[4]; f32x4 acc[8];
#pragma unroll
    for (int ks = 0; ks < 4; ++ks) A[ks] = *(LAS u32x4*)(lds + L_XN + c7 * XN_STRIDE + (k0 >> 1) + ks * 32 + ch * 16 + g * 4);
    tile_tmfma(acc, A, R);
    if (tile64_partial(acc, lds + L_P1 + u * 512, lds + L_FP, lane)) {
      const Tag tag = mk_tag(epoch_hash(ep));
#pragma unroll
      for (int i = 0; i < 2; ++i) {
        f32x4 s = {0.f, 0.f, 0.f, 0.f};
#pragma unroll
        for (int w = 0; w < 4; ++w) s += *(LAS f32x4*)(lds + L_P1 + w * 512 + i * 256 + lane * 4);
        st16_sc1(piece(pk_bf16(s[0], s[1]), pk_bf16(s[2], s[3]), tag), ws,
                 (tix < NFT ? WS_G + tix * 256 : WS_U + (tix - NFT) * 256) + (g * F + c * 4) * 4, i * 4 * F * 4);
      }
      FENCE();
      STAMP(10);
    }
  }
  if (wave == 7 || (wave == 6 && !t_q)) { STAMP_FLUSH(); return; }

  u32x4 W[16];
  f32x4 acc[4];
  const int voff = (8 * g * D + 8 * c) * 2;
  u64 z;
  if (wave >= 5) {
    f32x4 a8[8];
    z = 0;
    if (t_q) {
      const int f0 = (2 * (h - 16) + (wave - 5)) * FS;
      LAS unsigned* ai = lds + L_AI + (wave - 5) * 8 * AI_STRIDE;
      u32x4 R[32];
      const rsrc_t rw = mk_rsrc(a.wd + (size_t)f0 * D + b * 128, FS * D * 2);
      tile_load<32, WIN_S>(R, rw, voff, g >= 2 ? 0x7ffffff0 : voff, 32 * D * 2, D * 2);
      if (wave == 5) STAMP(19);
      const Tag tag = mk_tag(epoch_hash(ep));
      u32x4 v[8];
      gather(v, ws, WS_G + (gh * F + f0 + (lane & 31) * 4) * 4, (lane & 31) < FS / 4, tag, [](int i) { return i * 2 * F * 4; });
      if (wave == 5) STAMP(21);
#pragma unroll
      for (int i = 0; i < 4; ++i) {
        const u32x4 gt = v[i], up = v[4 + i];
        *(LAS u32x2*)(ai + (2 * i + gh) * AI_STRIDE + (lane & 31) * 2) =
            u32x2{pk_bf16(bf_lo(gt[0]) * bf_lo(up[0]), bf_hi(gt[0]) * bf_hi(up[0])), pk_bf16(bf_lo(gt[2]) * bf_lo(up[2]), bf_hi(gt[2]) * bf_hi(up[2]))};
      }
      LDS_DONE();
      u32x4 A[4];
#pragma unroll
      for (int ks = 0; ks < 4; ++ks) A[ks] = *(LAS u32x4*)(ai + c7 * AI_STRIDE + ks * 16 + g * 4);
      tile_tmfma(a8, A, R);
      if (wave == 6) {
#pragma unroll
        for (int e = 0; e < 8; ++e) *(LAS f32x4*)(lds + L_A6 + e * 256 + lane * 4) = a8[e];
        LDS_DONE();
        if (lane == 0) __hip_atomic_store(lds + L_F6, 1u, RLX_WG);
        STAMP_FLUSH();
        return;
      }
      lds_wait(lds + L_F6, 1u);
#pragma unroll
      for (int e = 0; e < 8; ++e) a8[e] += *(LAS f32x4*)(lds + L_A6 + e * 256 + lane * 4);
    } else {
#pragma unroll
      for (int e = 0; e < 8; ++e) a8[e] = f32x4{0.f, 0.f, 0.f, 0.f};
    }
    z = epoch_hash(ep);
#pragma unroll
    for (int e = 0; e < 4; ++e) { *(LAS f32x4*)(lds + L_ACC + e * 256 + lane * 4) = a8[e]; acc[e] = a8[4 + e]; }
    LDS_DONE();
    if (lane == 0) __hip_atomic_store(lds + L_FA, 1u, RLX_WG);
    lds_wait(lds + L_FW, 1u);
#pragma unroll
    for (int i = 0; i < 16; ++i) { W[i] = *(LAS u32x4*)(lds + L_WO + i * 256 + lane * 4); asm volatile("" : "+v"(W[i])); }
    STAMP(22);
    __builtin_amdgcn_s_setprio(2);
    lds_wait(lds + L_FO, 1u);
  } else {
    {
      u32x4 R[32];
      tile_load<32, WIN_S>(R, mk_rsrc(a.wo + (size_t)h * 128 * D + b * 128, 128 * D * 2), voff, voff, 32 * D * 2, D * 2);
#pragma unroll
      for (int ks = 0; ks < 4; ++ks)
#pragma unroll
        for (int e = 0; e < 4; ++e) {
          W[ks * 4 + e] = tile_frag(R, ks, e);
          asm volatile("" : "+v"(W[ks * 4 + e]));
          *(LAS u32x4*)(lds + L_WO + (ks * 4 + e) * 256 + lane * 4) = tile_frag(R, ks, 4 + e);
        }
      LDS_DONE();
      if (lane == 0) __hip_atomic_store(lds + L_FW, 1u, RLX_WG);
    }
    z = epoch_hash(ep);
    const Tag tag = mk_tag(z);
    STAMP(13);
    lds_wait(lds + L_FA, 1u);
#pragma unroll
    for (int e = 0; e < 4; ++e) { acc[e] = *(LAS f32x4*)(lds + L_ACC + e * 256 + lane * 4); asm volatile("" : "+v"(acc[e])); }
    __builtin_amdgcn_s_setprio(2);
    lds_wait(lds + L_FS, (unsigned)NS);
    STAMP(14);
    float mw[NS], lw[NS]; f32x2 aw[NS];
#pragma unroll
    for (int w = 0; w < NS; ++w) {
      const f32x2 ml = *(LAS f32x2*)(lds + L_REC + w * REC_STRIDE + 128);
      mw[w] = ml[0]; lw[w] = ml[1];
      aw[w] = *(LAS f32x2*)(lds + L_REC + w * REC_STRIDE + 2 * lane);
    }
    float M = mw[0];
#pragma unroll
    for (int w = 1; w < NS; ++w) M = fmaxf(M, mw[w]);
    float L = 0.f, o0 = 0.f, o1 = 0.f;
#pragma unroll
    for (int w = 0; w < NS; ++w) { const float f = __builtin_amdgcn_exp2f(mw[w] - M); L = fmaf(f, lw[w], L); o0 = fmaf(f, aw[w][0], o0); o1 = fmaf(f, aw[w][1], o1); }
    const float inv = __builtin_amdgcn_rcpf(L);
    const unsigned op = pk_bf16(div_by(o0, L, inv), div_by(o1, L, inv)), on = dppu<XOR1>(op);
    if ((lane & 1) == 0) st16_sc1(piece(op, on, tag), ws, WS_O + (h * 8 + b) * 512 + lane * 8, 0);
    STAMP(15);
    u32x4 v[4];
    gather(v, ws, WS_O + h * 4096 + lane * 16, true, tag, [](int i) { return i * 1024; });
    STAMP(16);
#pragma unroll
    for (int i = 0; i < 4; ++i) *(LAS u32x2*)(lds + L_AI + (2 * i + gh) * AI_STRIDE + (lane & 31) * 2) = u32x2{v[i][0], v[i][2]};
    LDS_DONE();
    if (lane == 0) __hip_atomic_store(lds + L_FO, 1u, RLX_WG);
    FENCE();
  }
  const Tag tag = mk_tag(z);
#pragma unroll
  for (int ks = 0; ks < 4; ++ks) {
    const u32x4 A = *(LAS u32x4*)(lds + L_AI + c7 * AI_STRIDE + ks * 16 + g * 4);
#pragma unroll
    for (int e = 0; e < 4; ++e) acc[e] = mfma(A, W[ks * 4 + e], acc[e]);
  }
#pragma unroll
  for (int r = 0; r < 4; ++r) {
    const float v0 = gh ? acc[2][r] : acc[0][r], v1 = gh ? acc[3][r] : acc[1][r];
    st16_sc1(piece(v0, v1, tag), ws,
             WS_P + ((b * 32 + 4 * g1 * 4 + (c >> 2)) * 32 + h) * 256 + u * 128 + ((c & 3) * 2 + gh) * 16, r * 4 * 32 * 256);
  }
  if (wave == 5) { STAMP_FLUSH(); return; }
  STAMP(17);
  {
    const int col = b * 128 + (h & 3) * 32 + (c7 >> 1) * 8 + ch * 4 + (c & 1) * 2;
    const unsigned xres = *(const GAS unsigned*)(a.x + (h >> 2) * D + col);
    u32x4 p[8];
    gather(p, ws, WS_P + (b * 32 + h) * 8192 + lane * 16, true, tag, [](int i) { return i * 1024; });
    float y0 = 0.f, y1 = 0.f;
#pragma unroll
    for (int i = 0; i < 8; ++i) { y0 += __uint_as_float(p[i][0]); y1 += __uint_as_float(p[i][2]); }
    y0 = xrow_sum(y0); y1 = xrow_sum(y1);
    if (g == 0) *(GAS unsigned*)(a.out + (h >> 2) * D + col) = pk_bf16(bf_lo(xres) + y0, bf_hi(xres) + y1);
  }
  if (wg == 0 && lane == 0) st8_sc1(u32x2{(unsigned)z, (unsigned)(z >> 32)}, ws, WS_EPOCH, 0);
  STAMP(18);
  STAMP_FLUSH();
}

}

extern "C" void kernel_launch(void* const* d_in, const int* in_sizes, int n_in, void* d_out, int out_size, void* d_ws, size_t ws_size,
                              hipStream_t stream) {
  Args a;
  a.x = (const unsigned short*)d_in[0];
  a.kc = (const unsigned short*)d_in[1];
  a.vc = (const unsigned short*)d_in[2];
  a.wq = (const unsigned short*)d_in[3];
  a.wo = (const unsigned short*)d_in[4];
  a.wg = (const unsigned short*)d_in[6];
  a.wu = (const unsigned short*)d_in[7];
  a.wd = (const unsigned short*)d_in[8];
  a.out = (unsigned short*)d_out;
  a.ws = (unsigned char*)d_ws;
  fused_decode<<<B * H, 512, 0, stream>>>(a);
}
```

```cpp
#include <hip/hip_runtime.h>
#include <hip/hip_bf16.h>
#include <stdint.h>

#define STAMPS 0
#define KV_T 4
#define POLL_SLEEP 2
#define WIN_Q 8
#define WIN_F 8
#define WIN_S 6
#define PRE 0
#define NX 0
#define WIN_K 8
#define WIN_KV 5
#define ABL 0
#define TROT 0
#define KV_T0 420
#define WMASK 0
#define WIN_KK(i) (WIN_KV + ((WMASK >> (i)) & 1))
#define WIN_VV(i) (WIN_KV + ((WMASK >> (4 + (i))) & 1))
#define TOUCH 1
#define SKEW 0
#define K_AUX 2
#define V_AUX 2
#define W_AUX 2
#define CODE_PF 21

namespace {

typedef unsigned u32x4 __attribute__((ext_vector_type(4)));
typedef unsigned u32x2 __attribute__((ext_vector_type(2)));
typedef float f32x4 __attribute__((ext_vector_type(4)));
typedef float f32x2 __attribute__((ext_vector_type(2)));
typedef __bf16 bf16x2 __attribute__((ext_vector_type(2)));
typedef __bf16 bf16x8 __attribute__((ext_vector_type(8)));
typedef unsigned long long u64;
#define GAS __attribute__((address_space(1)))
#define LAS __attribute__((address_space(3)))
#define RLX_WG __ATOMIC_RELAXED, __HIP_MEMORY_SCOPE_WORKGROUP
typedef __amdgpu_buffer_rsrc_t rsrc_t;

constexpr int B = 8, D = 1024, H = 32, SK = 4096, DH = 128, F = 3584;
constexpr int FS = F / 32;
constexpr int NQT = H * DH / 64, NFT = F / 64;
constexpr int AUX_NT = 2, AUX_SC1 = 16;
constexpr int NS = 4, T = KV_T;
constexpr int NT = (SK / 4) / (NS * T);

constexpr int WS_EPOCH = 0;
constexpr int WS_Q = 4096;
constexpr int WS_G = WS_Q + B * H * 2 * DH * 8;
constexpr int WS_U = WS_G + B * F * 4;
constexpr int WS_O = WS_U + B * F * 4;
constexpr int WS_P = WS_O + B * H * DH * 4;
constexpr int WS_END = WS_P + 256 * 8192;
constexpr int WS_STAMP = 16 << 20;

constexpr u64 SPIN_LIMIT = 2000000;

constexpr int XN_STRIDE = 516;
constexpr int L_XN = 0;
constexpr int L_P1 = L_XN + 8 * XN_STRIDE;
constexpr int L_A6 = L_P1 + 2048;
constexpr int L_ACC = L_A6 + 2048;
constexpr int L_WO = L_ACC + 1024;
constexpr int AI_STRIDE = 68;
constexpr int L_AI = L_WO + 4096;
constexpr int L_Q = L_AI + 16 * AI_STRIDE;
constexpr int L_REC = L_Q + 64;
constexpr int REC_STRIDE = 132;
constexpr int L_FX = L_REC + NS * REC_STRIDE;
constexpr int L_FP = L_FX + 1, L_FQ = L_FX + 2, L_FS = L_FX + 3, L_FA = L_FX + 4, L_FW = L_FX + 5, L_FO = L_FX + 6, L_F6 = L_FX + 7;
constexpr int L_END = L_FX + 8;

__device__ __forceinline__ float bf_lo(unsigned u) { return __uint_as_float(u << 16); }
__device__ __forceinline__ float bf_hi(unsigned u) { return __uint_as_float(u & 0xffff0000u); }
__device__ __forceinline__ unsigned pk_bf16(float lo, float hi) {
  const f32x2 v = {lo, hi};
  return __builtin_bit_cast(unsigned, __builtin_convertvector(v, bf16x2));
}
__device__ __forceinline__ float rbf(float x) { return bf_lo(pk_bf16(x, x)); }
__device__ __forceinline__ float dot2(unsigned k, unsigned q, float acc) {
  return __builtin_amdgcn_fdot2_f32_bf16(__builtin_bit_cast(bf16x2, k), __builtin_bit_cast(bf16x2, q), acc, false);
}
template <int CTRL> __device__ __forceinline__ unsigned dppu(unsigned x) { return (unsigned)__builtin_amdgcn_mov_dpp((int)x, CTRL, 0xf, 0xf, true); }
template <int CTRL> __device__ __forceinline__ float dpp(float x) { return __uint_as_float(dppu<CTRL>(__float_as_uint(x))); }
constexpr int XOR1 = 0xB1, XOR2 = 0x4E, XOR7 = 0x141, XOR8 = 0x128;
__device__ __forceinline__ float row16_sum(float d) {
  d += dpp<XOR1>(d); d += dpp<XOR2>(d); d += dpp<XOR7>(d); d += dpp<XOR8>(d);
  return d;
}
__device__ __forceinline__ float xrow_sum(float x) {
  auto s = __builtin_amdgcn_permlane16_swap(__float_as_uint(x), __float_as_uint(x), false, false);
  x = __uint_as_float(s[0]) + __uint_as_float(s[1]);
  auto t = __builtin_amdgcn_permlane32_swap(__float_as_uint(x), __float_as_uint(x), false, false);
  return __uint_as_float(t[0]) + __uint_as_float(t[1]);
}
__device__ __forceinline__ float xrow_max(float x) {
  auto s = __builtin_amdgcn_permlane16_swap(__float_as_uint(x), __float_as_uint(x), false, false);
  x = fmaxf(__uint_as_float(s[0]), __uint_as_float(s[1]));
  auto t = __builtin_amdgcn_permlane32_swap(__float_as_uint(x), __float_as_uint(x), false, false);
  return fmaxf(__uint_as_float(t[0]), __uint_as_float(t[1]));
}
__device__ __forceinline__ rsrc_t mk_rsrc(const void* p, unsigned bytes) {
  return __builtin_amdgcn_make_buffer_rsrc(const_cast<void*>(p), 0, bytes, 0x00020000);
}
template <int AUX> __device__ __forceinline__ u32x4 ld16(rsrc_t r, int voff, int soff) { return __builtin_amdgcn_raw_buffer_load_b128(r, voff, soff, AUX); }
__device__ __forceinline__ void st16_sc1(u32x4 v, rsrc_t r, int voff, int off) { __builtin_amdgcn_raw_buffer_store_b128(v, r, voff + off, 0, AUX_SC1); }
__device__ __forceinline__ void st8_sc1(u32x2 v, rsrc_t r, int voff, int soff) { __builtin_amdgcn_raw_buffer_store_b64(v, r, voff, soff, AUX_SC1); }
#define FENCE() asm volatile("" ::: "memory")
#define LDS_DONE() asm volatile("s_waitcnt lgkmcnt(0)" ::: "memory")
#define SCHED_FENCE() __builtin_amdgcn_sched_barrier(0)
template <int N> __device__ __forceinline__ void vm_wait() { asm volatile("s_waitcnt vmcnt(%0)" ::"n"(N) : "memory"); }
__device__ __forceinline__ void vm_wait_n(int n) {
  switch (n) { case 3: vm_wait<3>(); break; case 4: vm_wait<4>(); break; case 5: vm_wait<5>(); break; case 6: vm_wait<6>(); break; default: vm_wait<7>(); }
}
__device__ __forceinline__ u64 now() { return __builtin_amdgcn_s_memrealtime(); }

struct Tag { unsigned a, b; };
__device__ __forceinline__ bool fresh(const u32x4& v, const Tag& t) { return (v[1] == t.a) & (v[3] == t.b); }
__device__ __forceinline__ u32x4 piece(unsigned p0, unsigned p1, const Tag& t) { return u32x4{p0, t.a, p1, t.b}; }
__device__ __forceinline__ u32x4 piece(float p0, float p1, const Tag& t) { return u32x4{__float_as_uint(p0), t.a, __float_as_uint(p1), t.b}; }

template <int N> __device__ __forceinline__ void gather_init(u32x4 (&v)[N], bool act, const Tag& t) {
#pragma unroll
  for (int i = 0; i < N; ++i) v[i] = u32x4{0u, act ? ~t.a : t.a, 0u, t.b};
}
template <int N, class Off> __device__ __forceinline__ bool gather_pass(u32x4 (&v)[N], rsrc_t ws, int voff, const Tag& t, Off off) {
  FENCE();
#pragma unroll
  for (int i = 0; i < N; ++i)
    if (!fresh(v[i], t)) v[i] = ld16<AUX_SC1>(ws, voff, off(i));
  bool ok = true;
#pragma unroll
  for (int i = 0; i < N; ++i) ok &= fresh(v[i], t);
  const bool all = __all(ok);
  FENCE();
  return all;
}
template <int N, class Off> __device__ __forceinline__ void gather_wait(u32x4 (&v)[N], rsrc_t ws, int voff, const Tag& t, Off off) {
  const u64 t0 = now();
  while (!gather_pass(v, ws, voff, t, off)) {
    if (now() - t0 > SPIN_LIMIT) break;
    __builtin_amdgcn_s_sleep(POLL_SLEEP);
  }
}
template <int N, class Off> __device__ __forceinline__ void gather(u32x4 (&v)[N], rsrc_t ws, int voff, bool act, const Tag& t, Off off) {
  gather_init(v, act, t);
  gather_wait(v, ws, voff, t, off);
}
__device__ __forceinline__ u64 epoch_hash(u32x2 e) {
  asm volatile("" : "+v"(e));
  u64 z = (((u64)(unsigned)__builtin_amdgcn_readfirstlane(e[1]) << 32) | (unsigned)__builtin_amdgcn_readfirstlane(e[0])) + 0x9E3779B97F4A7C15ull;
  z = (z ^ (z >> 30)) * 0xBF58476D1CE4E5B9ull; z = (z ^ (z >> 27)) * 0x94D049BB133111EBull;
  return z ^ (z >> 31);
}
__device__ __forceinline__ Tag mk_tag(u64 z) { return Tag{(unsigned)z, (unsigned)(z >> 32)}; }
__device__ __forceinline__ void lds_wait(LAS unsigned* w, unsigned want) {
  const u64 t0 = now();
  while (__hip_atomic_load(w, RLX_WG) != want) {
    if (now() - t0 > SPIN_LIMIT) break;
    __builtin_amdgcn_s_sleep(1);
  }
  FENCE();
}

template <int N, int WIN> __device__ __forceinline__ void tile_load(u32x4 (&R)[N], rsrc_t r, int voff, int voff3, int step, int row) {
#pragma unroll
  for (int i = 0; i < N; ++i) {
    if (i >= WIN) vm_wait<WIN - 1>();
    R[i] = ld16<W_AUX>(r, i < 24 ? voff : voff3, (i >> 3) * step + (i & 7) * row);
    FENCE();
  }
}
template <int N> __device__ __forceinline__ u32x4 tile_frag(const u32x4 (&R)[N], int ks, int e) {
  u32x4 f;
#pragma unroll
  for (int p = 0; p < 4; ++p)
    f[p] = __builtin_amdgcn_perm(R[ks * 8 + 2 * p + 1][e >> 1], R[ks * 8 + 2 * p][e >> 1], (e & 1) ? 0x07060302u : 0x05040100u);
  return f;
}
__device__ __forceinline__ f32x4 mfma(const u32x4& a, const u32x4& b, const f32x4& c) {
  return __builtin_amdgcn_mfma_f32_16x16x32_bf16(__builtin_bit_cast(bf16x8, a), __builtin_bit_cast(bf16x8, b), c, 0, 0, 0);
}
template <int N> __device__ __forceinline__ void tile_tmfma(f32x4 (&acc)[8], const u32x4 (&A)[N / 8], const u32x4 (&R)[N]) {
#pragma unroll
  for (int e = 0; e < 8; ++e) acc[e] = f32x4{0.f, 0.f, 0.f, 0.f};
#pragma unroll
  for (int ks = 0; ks < N / 8; ++ks) {
#pragma unroll
    for (int e = 0; e < 8; ++e) acc[e] = mfma(A[ks], tile_frag(R, ks, e), acc[e]);
    SCHED_FENCE();
  }
}
__device__ __forceinline__ bool tile64_partial(const f32x4 (&acc)[8], LAS unsigned* part, LAS unsigned* counter, int lane) {
  const int gh = lane >> 5, g1 = (lane >> 4) & 1, ch = (lane >> 3) & 1, c7 = lane & 7;
  float y[2][8];
#pragma unroll
  for (int e = 0; e < 8; ++e)
#pragma unroll
    for (int r = 0; r < 2; ++r) {
      auto s = __builtin_amdgcn_permlane32_swap(__float_as_uint(acc[e][r]), __float_as_uint(acc[e][r + 2]), false, false);
      const float own = gh ? __uint_as_float(s[1]) : __uint_as_float(s[0]), other = gh ? __uint_as_float(s[0]) : __uint_as_float(s[1]);
      y[r][e] = own + dpp<XOR8>(other);
    }
  if (gh == ch) {
#pragma unroll
    for (int r = 0; r < 2; ++r) {
      LAS unsigned* d = part + (4 * g1 + 2 * gh + r) * 64 + c7 * 8;
      *(LAS f32x4*)d = f32x4{y[r][0], y[r][1], y[r][2], y[r][3]};
      *(LAS f32x4*)(d + 4) = f32x4{y[r][4], y[r][5], y[r][6], y[r][7]};
    }
  }
  LDS_DONE();
  unsigned arrived = 0;
  if (lane == 0) arrived = __hip_atomic_fetch_add(counter, 1u, RLX_WG);
  const bool last = __builtin_amdgcn_readfirstlane(arrived) == 3u;
  FENCE();
  return last;
}
__device__ __forceinline__ f32x2 tile64_sum(LAS unsigned* part, int i, int lane) {
  f32x2 s = {0.f, 0.f};
#pragma unroll
  for (int w = 0; w < 4; ++w) s += *(LAS f32x2*)(part + w * 512 + i * 128 + lane * 2);
  return s;
}

struct Args {
  const unsigned short *x, *kc, *vc, *wq, *wo, *wg, *wu, *wd;
  unsigned short* out;
  unsigned char* ws;
};

#define STAMP(i) do {} while (0)
#define STAMP_FLUSH() do {} while (0)

__device__ __forceinline__ float div_by(float a, float d, float rd) {
  const float q = a * rd;
  return fmaf(fmaf(-q, d, a), rd, q);
}
__device__ __forceinline__ void ln_row(const u32x4& x0, const u32x4& x1, LAS unsigned* dst, int lane) {
  float v[16];
#pragma unroll
  for (int k = 0; k < 4; ++k) { v[2 * k] = bf_lo(x0[k]); v[2 * k + 1] = bf_hi(x0[k]); v[8 + 2 * k] = bf_lo(x1[k]); v[8 + 2 * k + 1] = bf_hi(x1[k]); }
  float s1 = 0.f;
#pragma unroll
  for (int k = 0; k < 8; ++k) s1 += v[k] + v[8 + k];
  const float mean = xrow_sum(row16_sum(s1)) * (1.f / D);
  float s2 = 0.f;
#pragma unroll
  for (int k = 0; k < 16; ++k) s2 = fmaf(v[k] - mean, v[k] - mean, s2);
  const float var = xrow_sum(row16_sum(s2)) * (1.f / D);
  const float m = rbf(mean), den = rbf(sqrtf(rbf(rbf(var) + 1e-5f))), rden = __builtin_amdgcn_rcpf(den);
  u32x4 o0, o1;
#pragma unroll
  for (int k = 0; k < 4; ++k) {
    const unsigned d0 = pk_bf16(v[2 * k] - m, v[2 * k + 1] - m), d1 = pk_bf16(v[8 + 2 * k] - m, v[8 + 2 * k + 1] - m);
    o0[k] = pk_bf16(div_by(bf_lo(d0), den, rden), div_by(bf_hi(d0), den, rden));
    o1[k] = pk_bf16(div_by(bf_lo(d1), den, rden), div_by(bf_hi(d1), den, rden));
  }
  *(LAS u32x4*)(dst + lane * 4) = o0;
  *(LAS u32x4*)(dst + 256 + lane * 4) = o1;
}

__global__ void __launch_bounds__(512, 2) fused_decode(Args a) {
  __shared__ __attribute__((aligned(16))) unsigned lds_[L_END];
  LAS unsigned* lds = (LAS unsigned*)lds_;
  u64 pc; asm volatile("s_getpc_b64 %0" : "=s"(pc));
  asm volatile("" :: "s"(a.x), "s"(a.kc), "s"(a.vc), "s"(a.wq), "s"(a.wo), "s"(a.wg), "s"(a.wu), "s"(a.wd), "s"(a.out), "s"(a.ws));
  const int lane = threadIdx.x & 63, wave = __builtin_amdgcn_readfirstlane(threadIdx.x >> 6);
  const int g = lane >> 4, c = lane & 15, gh = g >> 1, g1 = g & 1, c7 = c & 7, ch = c >> 3;
  const int wg = blockIdx.x, b = wg >> 5, h = wg & 31;
  unsigned stampv = 0; (void)stampv;
  const rsrc_t ws = mk_rsrc(a.ws, WS_END);
  u32x2 ep;

  if (wave == 0) STAMP(0);
  const u64 t_entry = now();
  const bool t_ffn = h < 14, t_q = h >= 16;
  const int u = wave - NS;
  const int tb = (b + TROT) & 7;
  const int tix = t_ffn ? h * 8 + tb : (h - 16) * 8 + tb;
  const int ld2 = (t_ffn ? F : H * DH) * 2;
  const int k0 = t_ffn ? 256 * (u & 3) : 512 * ((tix >> 6) & 1) + 128 * (u & 3);
  const rsrc_t rw1 = mk_rsrc((const char*)(t_ffn ? (tix < NFT ? a.wg : a.wu) : a.wq) + (size_t)k0 * ld2 + (t_ffn ? (tix < NFT ? tix : tix - NFT) : (tix & 63)) * 128,
                             (wave >= NS && (t_ffn | t_q) && !(ABL & 1)) ? 256 * ld2 : 0);
  const int voff1 = (32 * ch + 8 * g) * ld2 + c7 * 16;
  u32x4 R0[8];
  {
    const rsrc_t rx = mk_rsrc(a.x, B * D * 2);
    if (wave >= NS) {
#pragma unroll
      for (int i = 0; i < 8; ++i) R0[i] = ld16<W_AUX>(rw1, voff1, i * ld2);
    } else {
#pragma unroll
      for (int i = 0; i < 4; ++i) R0[i] = ld16<0>(rx, wave * 2048 + lane * 16, (i & 1) * 1024 + (i >> 1) * 8192);
      R0[4] = R0[5] = R0[6] = R0[7] = u32x4{0u, 0u, 0u, 0u};
      R0[4] = ld16<0>(mk_rsrc((const void*)(pc & ~(u64)255), wave == 3 ? CODE_PF * 1024 : 0), lane * 16 + (wg >> 3) * 1024, 0);
      const unsigned n = wave == 2 ? 16u : 0u;
      R0[5][0] = __builtin_amdgcn_raw_buffer_load_b32(mk_rsrc(a.kc + (size_t)wg * SK * DH, n), 0, 0, AUX_NT);
      R0[5][1] = __builtin_amdgcn_raw_buffer_load_b32(mk_rsrc(a.vc + (size_t)wg * SK * DH, n), 0, 0, AUX_NT);
      R0[5][2] = __builtin_amdgcn_raw_buffer_load_b32(mk_rsrc(a.wo + (size_t)h * 128 * D + b * 128, n), 0, 0, AUX_NT);
      R0[5][3] = __builtin_amdgcn_raw_buffer_load_b32(mk_rsrc(a.wd + (size_t)(h & 15) * 2 * FS * D + b * 128, n), 0, 0, AUX_NT);
      R0[6][0] = __builtin_amdgcn_raw_buffer_load_b32(mk_rsrc(a.ws + WS_Q + wg * 2048, n), 0, 0, AUX_NT);
      R0[6][1] = __builtin_amdgcn_raw_buffer_load_b32(mk_rsrc(a.ws + WS_P + wg * 8192, n), 0, 0, AUX_NT);
      R0[6][2] = __builtin_amdgcn_raw_buffer_load_b32(mk_rsrc(a.out, n), 0, 0, AUX_NT);
    }
    ep = __builtin_amdgcn_raw_buffer_load_b64(ws, WS_EPOCH, 0, 0);
    FENCE();
    if (wave == 0) { if (lane < 8) lds[L_FX + lane] = 0u; LDS_DONE(); }
    __builtin_amdgcn_s_barrier();
    FENCE();
  }

  if (wave < NS) {
    const rsrc_t rk = mk_rsrc(a.kc + (size_t)wg * SK * DH, SK * DH * 2);
    const rsrc_t rv = mk_rsrc(a.vc + (size_t)wg * SK * DH, SK * DH * 2);
    const int kvoff = lane * 16 + wave * T * 1024;
    const int rot = (SKEW) & (NT - 1);
    auto toff = [&](int tt) { return ((tt + rot) & (NT - 1)) * (NS * T * 1024); };
    if (t_ffn | t_q) {
#pragma unroll
      for (int i = 0; i < 2; ++i) ln_row(R0[2 * i], R0[2 * i + 1], lds + L_XN + (wave + 4 * i) * XN_STRIDE, lane);
      LDS_DONE();
      if (lane == 0) __hip_atomic_fetch_add(lds + L_FX, 1u, RLX_WG);
      FENCE();
    }
    asm volatile("" :: "v"(R0[4]), "v"(R0[5]), "v"(R0[6]));
    if ((t_ffn | t_q) && !(ABL & 1)) {
      const u64 limit = t_q ? SPIN_LIMIT : KV_T0;
      while (__hip_atomic_load(lds + L_FP, RLX_WG) != 4u && now() - t_entry < limit) __builtin_amdgcn_s_sleep(2);
      FENCE();
    }
    u32x4 Kr[T], Vr[T], X[NX + 1][2 * T];
#pragma unroll
    for (int i = 0; i < 2 * T; ++i) {
      if (i >= WIN_K) vm_wait<(WIN_K > 0 ? WIN_K - 1 : 0)>();
      (i < T ? Kr[i] : Vr[i - T]) = ld16<AUX_NT>(i < T ? rk : rv, kvoff + (i % T) * 1024, toff(0));
      FENCE();
    }
    int nx = 0;
    {
      const Tag tag = mk_tag(epoch_hash(ep));
      u32x4 v[2];
      gather_init(v, wave == 0, tag);
      auto qoff = [](int i) { return i * 1024; };
      const int qo = WS_Q + wg * 2048 + lane * 16;
      bool have = ABL & 1;
#pragma unroll
      for (int j = 0; j < NX; ++j) {
        if (!have) {
#pragma unroll
          for (int i = 0; i < 2 * T; ++i) {
            vm_wait<WIN_K - 1>();
            X[j][i] = ld16<AUX_NT>(i < T ? rk : rv, kvoff + (i % T) * 1024, toff(j + 1));
            FENCE();
          }
          nx = j + 1;
          have = wave == 0 ? gather_pass(v, ws, qo, tag, qoff) : __hip_atomic_load(lds + L_FQ, RLX_WG) == 1u;
        }
      }
      if (ABL & 1) {
      } else if (wave == 0) {
        if (!have) gather_wait(v, ws, qo, tag, qoff);
        lds[L_Q + lane] = pk_bf16(__uint_as_float(v[0][0]) + __uint_as_float(v[1][0]), __uint_as_float(v[0][2]) + __uint_as_float(v[1][2]));
        LDS_DONE();
        if (lane == 0) __hip_atomic_store(lds + L_FQ, 1u, RLX_WG);
        FENCE();
        STAMP(3);
      } else {
        lds_wait(lds + L_FQ, 1u);
      }
    }
    const u32x4 qv = *(LAS u32x4*)(lds + L_Q + c * 4);
    constexpr float SCALE = 0.08838834764831845f, LOG2E = 1.4426950408889634f;
    float m = -INFINITY, l = 0.f, acc[8];
#pragma unroll
    for (int k = 0; k < 8; ++k) acc[k] = 0.f;
    auto step = [&](u32x4* Kt, u32x4* Vt, const bool more, const int so) __attribute__((always_inline)) {
      float s[T];
#pragma unroll
      for (int i = 0; i < T; ++i) {
        float d = 0.f;
#pragma unroll
        for (int k = 0; k < 4; ++k) d = dot2(Kt[i][k], qv[k], d);
        asm volatile("" : "+v"(d));
        s[i] = d;
        SCHED_FENCE();
        if (more) { if (WIN_KK(i) < 2 * T) vm_wait_n(WIN_KK(i) - 1); Kt[i] = ld16<K_AUX>(rk, kvoff + i * 1024, so); FENCE(); }
        SCHED_FENCE();
      }
      float mx = m;
#pragma unroll
      for (int i = 0; i < T; ++i) {
        s[i] = rbf(rbf(row16_sum(s[i])) * SCALE) * LOG2E;
        mx = fmaxf(mx, s[i]);
      }
      const float alpha = __builtin_amdgcn_exp2f(m - mx);
      m = mx; l *= alpha;
#pragma unroll
      for (int k = 0; k < 8; ++k) acc[k] *= alpha;
#pragma unroll
      for (int i = 0; i < T; ++i) { s[i] = __builtin_amdgcn_exp2f(s[i] - mx); l += s[i]; }
      SCHED_FENCE();
#pragma unroll
      for (int i = 0; i < T; ++i) {
#pragma unroll
        for (int k = 0; k < 4; ++k) { acc[2 * k] = fmaf(s[i], bf_lo(Vt[i][k]), acc[2 * k]); acc[2 * k + 1] = fmaf(s[i], bf_hi(Vt[i][k]), acc[2 * k + 1]); }
#pragma unroll
        for (int k = 0; k < 8; k += 2) { f32x2 p2 = {acc[k], acc[k + 1]}; asm volatile("" : "+v"(p2)); acc[k] = p2[0]; acc[k + 1] = p2[1]; }
        SCHED_FENCE();
        if (more) { if (WIN_VV(i) < 2 * T) vm_wait_n(WIN_VV(i) - 1); Vt[i] = ld16<V_AUX>(rv, kvoff + i * 1024, so); FENCE(); }
        SCHED_FENCE();
      }
    };
    step(Kr, Vr, true, toff(nx + 1));
#pragma unroll
    for (int j = 0; j < NX; ++j)
      if (j < nx) step(&X[j][0], &X[j][T], false, 0);
    for (int tt = nx + 2; tt < NT; ++tt) step(Kr, Vr, true, toff(tt));
    step(Kr, Vr, false, 0);
    STAMP(4 + wave);
    const float M = xrow_max(m), f = __builtin_amdgcn_exp2f(m - M);
    l = xrow_sum(l * f);
#pragma unroll
    for (int k = 0; k < 8; ++k) acc[k] = xrow_sum(acc[k] * f);
    LAS unsigned* rec = lds + L_REC + wave * REC_STRIDE;
    if (g == 0) {
      *(LAS f32x4*)(rec + 8 * c) = f32x4{acc[0], acc[1], acc[2], acc[3]};
      *(LAS f32x4*)(rec + 8 * c + 4) = f32x4{acc[4], acc[5], acc[6], acc[7]};
      if (c == 0) *(LAS f32x2*)(rec + 128) = f32x2{M, l};
    }
    LDS_DONE();
    if (lane == 0) __hip_atomic_fetch_add(lds + L_FS, 1u, RLX_WG);
    STAMP_FLUSH();
    return;
  }

  if (ABL & 1) return;
  if (t_q) {
    u32x4 R[16];
#pragma unroll
    for (int i = 0; i < 8; ++i) { R[i] = R0[i]; vm_wait<WIN_Q - 1>(); R[8 + i] = ld16<W_AUX>(rw1, voff1, (64 + i) * ld2); FENCE(); }
    lds_wait(lds + L_FX, (unsigned)NS);
    u32x4 A[2]; f32x4 acc[8];
#pragma unroll
    for (int ks = 0; ks < 2; ++ks) A[ks] = *(LAS u32x4*)(lds + L_XN + c7 * XN_STRIDE + (k0 >> 1) + ks * 32 + ch * 16 + g * 4);
    tile_tmfma(acc, A, R);
    if (tile64_partial(acc, lds + L_P1 + u * 512, lds + L_FP, lane)) {
      const Tag tag = mk_tag(epoch_hash(ep));
#pragma unroll
      for (int i = 0; i < 4; ++i) {
        const f32x2 s = tile64_sum(lds + L_P1, i, lane);
        st16_sc1(piece(s[0], s[1], tag), ws,
                 WS_Q + (gh * 32 + ((tix & 63) >> 1)) * 2048 + (tix >> 6) * 1024 + (tix & 1) * 512 + (lane & 31) * 16, i * 2 * 32 * 2048);
      }
      FENCE();
      STAMP(9);
    }
  } else if (t_ffn) {
    u32x4 R[32];
#pragma unroll
    for (int i = 0; i < 8; ++i) R[i] = R0[i];
#pragma unroll
    for (int i = 8; i < 32; ++i) { vm_wait<WIN_F - 1>(); R[i] = ld16<W_AUX>(rw1, voff1, ((i >> 3) * 64 + (i & 7)) * ld2); FENCE(); }
    if (wave == 4) STAMP(34);
    lds_wait(lds + L_FX, (unsigned)NS);
    u32x4 A[4]; f32x4 acc[8];
#pragma unroll
    for (int ks = 0; ks < 4; ++ks) A[ks] = *(LAS u32x4*)(lds + L_XN + c7 * XN_STRIDE + (k0 >> 1) + ks * 32 + ch * 16 + g * 4);
    tile_tmfma(acc, A, R);
    if (tile64_partial(acc, lds + L_P1 + u * 512, lds + L_FP, lane)) {
      const Tag tag = mk_tag(epoch_hash(ep));
#pragma unroll
      for (int i = 0; i < 2; ++i) {
        f32x4 s = {0.f, 0.f, 0.f, 0.f};
#pragma unroll
        for (int w = 0; w < 4; ++w) s += *(LAS f32x4*)(lds + L_P1 + w * 512 + i * 256 + lane * 4);
        st16_sc1(piece(pk_bf16(s[0], s[1]), pk_bf16(s[2], s[3]), tag), ws,
                 (tix < NFT ? WS_G + tix * 256 : WS_U + (tix - NFT) * 256) + (g * F + c * 4) * 4, i * 4 * F * 4);
      }
      FENCE();
      STAMP(10);
    }
  }
  if (wave == 7 || (wave == 6 && !t_q)) { STAMP_FLUSH(); return; }

  u32x4 W[16];
  f32x4 acc[4];
  const int voff = (8 * g * D + 8 * c) * 2;
  u64 z;
  if (wave >= 5) {
    f32x4 a8[8];
    z = 0;
    if (t_q) {
      const int f0 = (2 * (h - 16) + (wave - 5)) * FS;
      LAS unsigned* ai = lds + L_AI + (wave - 5) * 8 * AI_STRIDE;
      u32x4 R[32];
      const rsrc_t rw = mk_rsrc(a.wd + (size_t)f0 * D + b * 128, FS * D * 2);
      tile_load<32, WIN_S>(R, rw, voff, g >= 2 ? 0x7ffffff0 : voff, 32 * D * 2, D * 2);
      if (wave == 5) STAMP(19);
      const Tag tag = mk_tag(epoch_hash(ep));
      u32x4 v[8];
      gather(v, ws, WS_G + (gh * F + f0 + (lane & 31) * 4) * 4, (lane & 31) < FS / 4, tag, [](int i) { return i * 2 * F * 4; });
      if (wave == 5) STAMP(21);
#pragma unroll
      for (int i = 0; i < 4; ++i) {
        const u32x4 gt = v[i], up = v[4 + i];
        *(LAS u32x2*)(ai + (2 * i + gh) * AI_STRIDE + (lane & 31) * 2) =
            u32x2{pk_bf16(bf_lo(gt[0]) * bf_lo(up[0]), bf_hi(gt[0]) * bf_hi(up[0])), pk_bf16(bf_lo(gt[2]) * bf_lo(up[2]), bf_hi(gt[2]) * bf_hi(up[2]))};
      }
      LDS_DONE();
      u32x4 A[4];
#pragma unroll
      for (int ks = 0; ks < 4; ++ks) A[ks] = *(LAS u32x4*)(ai + c7 * AI_STRIDE + ks * 16 + g * 4);
      tile_tmfma(a8, A, R);
      if (wave == 6) {
#pragma unroll
        for (int e = 0; e < 8; ++e) *(LAS f32x4*)(lds + L_A6 + e * 256 + lane * 4) = a8[e];
        LDS_DONE();
        if (lane == 0) __hip_atomic_store(lds + L_F6, 1u, RLX_WG);
        STAMP_FLUSH();
        return;
      }
      lds_wait(lds + L_F6, 1u);
#pragma unroll
      for (int e = 0; e < 8; ++e) a8[e] += *(LAS f32x4*)(lds + L_A6 + e * 256 + lane * 4);
    } else {
#pragma unroll
      for (int e = 0; e < 8; ++e) a8[e] = f32x4{0.f, 0.f, 0.f, 0.f};
    }
    z = epoch_hash(ep);
#pragma unroll
    for (int e = 0; e < 4; ++e) { *(LAS f32x4*)(lds + L_ACC + e * 256 + lane * 4) = a8[e]; acc[e] = a8[4 + e]; }
    LDS_DONE();
    if (lane == 0) __hip_atomic_store(lds + L_FA, 1u, RLX_WG);
    lds_wait(lds + L_FW, 1u);
#pragma unroll
    for (int i = 0; i < 16; ++i) { W[i] = *(LAS u32x4*)(lds + L_WO + i * 256 + lane * 4); asm volatile("" : "+v"(W[i])); }
    STAMP(22);
    __builtin_amdgcn_s_setprio(2);
    lds_wait(lds + L_FO, 1u);
  } else {
    {
      u32x4 R[32];
      tile_load<32, WIN_S>(R, mk_rsrc(a.wo + (size_t)h * 128 * D + b * 128, 128 * D * 2), voff, voff, 32 * D * 2, D * 2);
#pragma unroll
      for (int ks = 0; ks < 4; ++ks)
#pragma unroll
        for (int e = 0; e < 4; ++e) {
          W[ks * 4 + e] = tile_frag(R, ks, e);
          asm volatile("" : "+v"(W[ks * 4 + e]));
          *(LAS u32x4*)(lds + L_WO + (ks * 4 + e) * 256 + lane * 4) = tile_frag(R, ks, 4 + e);
        }
      LDS_DONE();
      if (lane == 0) __hip_atomic_store(lds + L_FW, 1u, RLX_WG);
    }
    z = epoch_hash(ep);
    const Tag tag = mk_tag(z);
    STAMP(13);
    lds_wait(lds + L_FA, 1u);
#pragma unroll
    for (int e = 0; e < 4; ++e) { acc[e] = *(LAS f32x4*)(lds + L_ACC + e * 256 + lane * 4); asm volatile("" : "+v"(acc[e])); }
    __builtin_amdgcn_s_setprio(2);
    lds_wait(lds + L_FS, (unsigned)NS);
    STAMP(14);
    float mw[NS], lw[NS]; f32x2 aw[NS];
#pragma unroll
    for (int w = 0; w < NS; ++w) {
      const f32x2 ml = *(LAS f32x2*)(lds + L_REC + w * REC_STRIDE + 128);
      mw[w] = ml[0]; lw[w] = ml[1];
      aw[w] = *(LAS f32x2*)(lds + L_REC + w * REC_STRIDE + 2 * lane);
    }
    float M = mw[0];
#pragma unroll
    for (int w = 1; w < NS; ++w) M = fmaxf(M, mw[w]);
    float L = 0.f, o0 = 0.f, o1 = 0.f;
#pragma unroll
    for (int w = 0; w < NS; ++w) { const float f = __builtin_amdgcn_exp2f(mw[w] - M); L = fmaf(f, lw[w], L); o0 = fmaf(f, aw[w][0], o0); o1 = fmaf(f, aw[w][1], o1); }
    const float inv = __builtin_amdgcn_rcpf(L);
    const unsigned op = pk_bf16(div_by(o0, L, inv), div_by(o1, L, inv)), on = dppu<XOR1>(op);
    if ((lane & 1) == 0) st16_sc1(piece(op, on, tag), ws, WS_O + (h * 8 + b) * 512 + lane * 8, 0);
    STAMP(15);
    u32x4 v[4];
    gather(v, ws, WS_O + h * 4096 + lane * 16, true, tag, [](int i) { return i * 1024; });
    STAMP(16);
#pragma unroll
    for (int i = 0; i < 4; ++i) *(LAS u32x2*)(lds + L_AI + (2 * i + gh) * AI_STRIDE + (lane & 31) * 2) = u32x2{v[i][0], v[i][2]};
    LDS_DONE();
    if (lane == 0) __hip_atomic_store(lds + L_FO, 1u, RLX_WG);
    FENCE();
  }
  const Tag tag = mk_tag(z);
#pragma unroll
  for (int ks = 0; ks < 4; ++ks) {
    const u32x4 A = *(LAS u32x4*)(lds + L_AI + c7 * AI_STRIDE + ks * 16 + g * 4);
#pragma unroll
    for (int e = 0; e < 4; ++e) acc[e] = mfma(A, W[ks * 4 + e], acc[e]);
  }
#pragma unroll
  for (int r = 0; r < 4; ++r) {
    const float v0 = gh ? acc[2][r] : acc[0][r], v1 = gh ? acc[3][r] : acc[1][r];
    st16_sc1(piece(v0, v1, tag), ws,
             WS_P + ((b * 32 + 4 * g1 * 4 + (c >> 2)) * 32 + h) * 256 + u * 128 + ((c & 3) * 2 + gh) * 16, r * 4 * 32 * 256);
  }
  if (wave == 5) { STAMP_FLUSH(); return; }
  STAMP(17);
  {
    const int col = b * 128 + (h & 3) * 32 + (c7 >> 1) * 8 + ch * 4 + (c & 1) * 2;
    const unsigned xres = *(const GAS unsigned*)(a.x + (h >> 2) * D + col);
    u32x4 p[8];
    gather(p, ws, WS_P + (b * 32 + h) * 8192 + lane * 16, true, tag, [](int i) { return i * 1024; });
    float y0 = 0.f, y1 = 0.f;
#pragma unroll
    for (int i = 0; i < 8; ++i) { y0 += __uint_as_float(p[i][0]); y1 += __uint_as_float(p[i][2]); }
    y0 = xrow_sum(y0); y1 = xrow_sum(y1);
    if (g == 0) *(GAS unsigned*)(a.out + (h >> 2) * D + col) = pk_bf16(bf_lo(xres) + y0, bf_hi(xres) + y1);
  }
  if (wg == 0 && lane == 0) st8_sc1(u32x2{(unsigned)z, (unsigned)(z >> 32)}, ws, WS_EPOCH, 0);
  STAMP(18);
  STAMP_FLUSH();
}

}

extern "C" void kernel_launch(void* const* d_in, const int* in_sizes, int n_in, void* d_out, int out_size, void* d_ws, size_t ws_size,
                              hipStream_t stream) {
  Args a;
  a.x = (const unsigned short*)d_in[0];
  a.kc = (const unsigned short*)d_in[1];
  a.vc = (const unsigned short*)d_in[2];
  a.wq = (const unsigned short*)d_in[3];
  a.wo = (const unsigned short*)d_in[4];
  a.wg = (const unsigned short*)d_in[6];
  a.wu = (const unsigned short*)d_in[7];
  a.wd = (const unsigned short*)d_in[8];
  a.out = (unsigned short*)d_out;
  a.ws = (unsigned char*)d_ws;
  fused_decode<<<B * H, 512, 0, stream>>>(a);
}
```

```cpp
#include <hip/hip_runtime.h>
#include <hip/hip_bf16.h>
#include <stdint.h>

#define LDS_PAD 15000
#define STAMPS 0
#define KV_T 4
#define POLL_SLEEP 2
#define WIN_Q 8
#define WIN_F 8
#define WIN_S 6
#define PRE 0
#define NX 0
#define WIN_K 8
#define WIN_KV 5
#define ABL 0
#define TROT 0
#define KV_T0 420
#define WMASK 0
#define WIN_KK(i) (WIN_KV + ((WMASK >> (i)) & 1))
#define WIN_VV(i) (WIN_KV + ((WMASK >> (4 + (i))) & 1))
#define TOUCH 1
#define SKEW 0
#define K_AUX 2
#define V_AUX 2
#define W_AUX 2
#define LSLEEP 16
#define DBGOUT 0
#define DBGWG 0
#define DBGSUB 0.f
#define CODE_PF 1

namespace {

typedef unsigned u32x4 __attribute__((ext_vector_type(4)));
typedef unsigned u32x2 __attribute__((ext_vector_type(2)));
typedef float f32x4 __attribute__((ext_vector_type(4)));
typedef float f32x2 __attribute__((ext_vector_type(2)));
typedef __bf16 bf16x2 __attribute__((ext_vector_type(2)));
typedef __bf16 bf16x8 __attribute__((ext_vector_type(8)));
typedef unsigned long long u64;
#define GAS __attribute__((address_space(1)))
#define LAS __attribute__((address_space(3)))
#define RLX_WG __ATOMIC_RELAXED, __HIP_MEMORY_SCOPE_WORKGROUP
typedef __amdgpu_buffer_rsrc_t rsrc_t;

constexpr int B = 8, D = 1024, H = 32, SK = 4096, DH = 128, F = 3584;
constexpr int FS = F / 32;
constexpr int NQT = H * DH / 64, NFT = F / 64;
constexpr int AUX_NT = 2, AUX_SC1 = 16;
constexpr int NS = 4;
constexpr int NSTEP = SK / (NS * 32);

constexpr int WS_EPOCH = 0;
constexpr int WS_Q = 4096;
constexpr int WS_G = WS_Q + B * H * 2 * DH * 8;
constexpr int WS_U = WS_G + B * F * 4;
constexpr int WS_O = WS_U + B * F * 4;
constexpr int WS_P = WS_O + B * H * DH * 4;
constexpr int WS_END = WS_P + 256 * 8192;
constexpr int WS_STAMP = 16 << 20;

constexpr unsigned SPIN_COUNT = 1u << 19;
constexpr u64 SPIN_LIMIT = 2000000;

constexpr int XN_STRIDE = 516;
constexpr int L_XN = 0;
constexpr int L_P1 = L_XN + 8 * XN_STRIDE;
constexpr int L_A6 = L_P1 + 2048;
constexpr int L_ACC = L_A6 + 2048;
constexpr int L_WO = L_ACC + 1024;
constexpr int AI_STRIDE = 68;
constexpr int L_AI = L_WO + 4096;
constexpr int L_Q = L_AI + 16 * AI_STRIDE;
constexpr int L_S = L_Q + 64;
constexpr int L_REC = L_S + NS * NSTEP * 64;
constexpr int REC_STRIDE = 132;
constexpr int L_FX = L_REC + NS * REC_STRIDE;
constexpr int L_FP = L_FX + 1, L_FQ = L_FX + 2, L_FS = L_FX + 3, L_FA = L_FX + 4, L_FW = L_FX + 5, L_FO = L_FX + 6, L_F6 = L_FX + 7, L_FE = L_FX + 8, L_DBG = L_FX + 9;
constexpr int L_END = L_FX + 16;

__device__ __forceinline__ float bf_lo(unsigned u) { return __uint_as_float(u << 16); }
__device__ __forceinline__ float bf_hi(unsigned u) { return __uint_as_float(u & 0xffff0000u); }
__device__ __forceinline__ unsigned pk_bf16(float lo, float hi) {
  const f32x2 v = {lo, hi};
  return __builtin_bit_cast(unsigned, __builtin_convertvector(v, bf16x2));
}
__device__ __forceinline__ float rbf(float x) { return bf_lo(pk_bf16(x, x)); }
__device__ __forceinline__ float dot2(unsigned k, unsigned q, float acc) {
  return __builtin_amdgcn_fdot2_f32_bf16(__builtin_bit_cast(bf16x2, k), __builtin_bit_cast(bf16x2, q), acc, false);
}
template <int CTRL> __device__ __forceinline__ unsigned dppu(unsigned x) { return (unsigned)__builtin_amdgcn_mov_dpp((int)x, CTRL, 0xf, 0xf, true); }
template <int CTRL> __device__ __forceinline__ float dpp(float x) { return __uint_as_float(dppu<CTRL>(__float_as_uint(x))); }
constexpr int XOR1 = 0xB1, XOR2 = 0x4E, XOR7 = 0x141, XOR8 = 0x128;
__device__ __forceinline__ float row16_sum(float d) {
  d += dpp<XOR1>(d); d += dpp<XOR2>(d); d += dpp<XOR7>(d); d += dpp<XOR8>(d);
  return d;
}
__device__ __forceinline__ float xrow_sum(float x) {
  auto s = __builtin_amdgcn_permlane16_swap(__float_as_uint(x), __float_as_uint(x), false, false);
  x = __uint_as_float(s[0]) + __uint_as_float(s[1]);
  auto t = __builtin_amdgcn_permlane32_swap(__float_as_uint(x), __float_as_uint(x), false, false);
  return __uint_as_float(t[0]) + __uint_as_float(t[1]);
}
__device__ __forceinline__ float xrow_max(float x) {
  auto s = __builtin_amdgcn_permlane16_swap(__float_as_uint(x), __float_as_uint(x), false, false);
  x = fmaxf(__uint_as_float(s[0]), __uint_as_float(s[1]));
  auto t = __builtin_amdgcn_permlane32_swap(__float_as_uint(x), __float_as_uint(x), false, false);
  return fmaxf(__uint_as_float(t[0]), __uint_as_float(t[1]));
}
__device__ __forceinline__ rsrc_t mk_rsrc(const void* p, unsigned bytes) {
  return __builtin_amdgcn_make_buffer_rsrc(const_cast<void*>(p), 0, bytes, 0x00020000);
}
template <int AUX> __device__ __forceinline__ u32x4 ld16(rsrc_t r, int voff, int soff) { return __builtin_amdgcn_raw_buffer_load_b128(r, voff, soff, AUX); }
__device__ __forceinline__ void st16_sc1(u32x4 v, rsrc_t r, int voff, int off) { __builtin_amdgcn_raw_buffer_store_b128(v, r, voff + off, 0, AUX_SC1); }
__device__ __forceinline__ void st8_sc1(u32x2 v, rsrc_t r, int voff, int soff) { __builtin_amdgcn_raw_buffer_store_b64(v, r, voff, soff, AUX_SC1); }
#define FENCE() asm volatile("" ::: "memory")
#define LDS_DONE() asm volatile("s_waitcnt lgkmcnt(0)" ::: "memory")
#define SCHED_FENCE() __builtin_amdgcn_sched_barrier(0)
template <int N> __device__ __forceinline__ void vm_wait() { asm volatile("s_waitcnt vmcnt(%0)" ::"n"(N) : "memory"); }
__device__ __forceinline__ void vm_wait_n(int n) {
  switch (n) { case 3: vm_wait<3>(); break; case 4: vm_wait<4>(); break; case 5: vm_wait<5>(); break; case 6: vm_wait<6>(); break; default: vm_wait<7>(); }
}
__device__ __forceinline__ u64 now() { return __builtin_amdgcn_s_memrealtime(); }

struct Tag { unsigned a, b; };
__device__ __forceinline__ bool fresh(const u32x4& v, const Tag& t) { return (v[1] == t.a) & (v[3] == t.b); }
__device__ __forceinline__ u32x4 piece(unsigned p0, unsigned p1, const Tag& t) { return u32x4{p0, t.a, p1, t.b}; }
__device__ __forceinline__ u32x4 piece(float p0, float p1, const Tag& t) { return u32x4{__float_as_uint(p0), t.a, __float_as_uint(p1), t.b}; }

template <int N> __device__ __forceinline__ void gather_init(u32x4 (&v)[N], bool act, const Tag& t) {
#pragma unroll
  for (int i = 0; i < N; ++i) v[i] = u32x4{0u, act ? ~t.a : t.a, 0u, t.b};
}
template <int N, class Off> __device__ __forceinline__ bool gather_pass(u32x4 (&v)[N], rsrc_t ws, int voff, const Tag& t, Off off) {
  FENCE();
#pragma unroll
  for (int i = 0; i < N; ++i)
    if (!fresh(v[i], t)) v[i] = ld16<AUX_SC1>(ws, voff, off(i));
  bool ok = true;
#pragma unroll
  for (int i = 0; i < N; ++i) ok &= fresh(v[i], t);
  const bool all = __all(ok);
  FENCE();
  return all;
}
template <int N, class Off> __device__ __forceinline__ void gather_wait(u32x4 (&v)[N], rsrc_t ws, int voff, const Tag& t, Off off) {
  const u64 t0 = now();
  while (!gather_pass(v, ws, voff, t, off)) {
    if (now() - t0 > SPIN_LIMIT) break;
    __builtin_amdgcn_s_sleep(POLL_SLEEP);
  }
}
template <int N, class Off> __device__ __forceinline__ void gather(u32x4 (&v)[N], rsrc_t ws, int voff, bool act, const Tag& t, Off off) {
  gather_init(v, act, t);
  gather_wait(v, ws, voff, t, off);
}
__device__ __forceinline__ u64 epoch_hash(u32x2 e) {
  asm volatile("" : "+v"(e));
  u64 z = (((u64)(unsigned)__builtin_amdgcn_readfirstlane(e[1]) << 32) | (unsigned)__builtin_amdgcn_readfirstlane(e[0])) + 0x9E3779B97F4A7C15ull;
  z = (z ^ (z >> 30)) * 0xBF58476D1CE4E5B9ull; z = (z ^ (z >> 27)) * 0x94D049BB133111EBull;
  return z ^ (z >> 31);
}
__device__ __forceinline__ Tag mk_tag(u64 z) { return Tag{(unsigned)z, (unsigned)(z >> 32)}; }
template <int SLEEP = 1> __device__ __forceinline__ void lds_wait(LAS unsigned* w, unsigned want) {
  for (unsigned n = 0; __hip_atomic_load(w, RLX_WG) != want && n < SPIN_COUNT / SLEEP; ++n) __builtin_amdgcn_s_sleep(SLEEP);
  FENCE();
}

template <int N, int WIN> __device__ __forceinline__ void tile_load(u32x4 (&R)[N], rsrc_t r, int voff, int voff3, int step, int row) {
#pragma unroll
  for (int i = 0; i < N; ++i) {
    if (i >= WIN) vm_wait<WIN - 1>();
    R[i] = ld16<W_AUX>(r, i < 24 ? voff : voff3, (i >> 3) * step + (i & 7) * row);
    FENCE();
  }
}
template <int N> __device__ __forceinline__ u32x4 tile_frag(const u32x4 (&R)[N], int ks, int e) {
  u32x4 f;
#pragma unroll
  for (int p = 0; p < 4; ++p)
    f[p] = __builtin_amdgcn_perm(R[ks * 8 + 2 * p + 1][e >> 1], R[ks * 8 + 2 * p][e >> 1], (e & 1) ? 0x07060302u : 0x05040100u);
  return f;
}
__device__ __forceinline__ f32x4 mfma(const u32x4& a, const u32x4& b, const f32x4& c) {
  return __builtin_amdgcn_mfma_f32_16x16x32_bf16(__builtin_bit_cast(bf16x8, a), __builtin_bit_cast(bf16x8, b), c, 0, 0, 0);
}
template <int N> __device__ __forceinline__ void tile_tmfma(f32x4 (&acc)[8], const u32x4 (&A)[N / 8], const u32x4 (&R)[N]) {
#pragma unroll
  for (int e = 0; e < 8; ++e) acc[e] = f32x4{0.f, 0.f, 0.f, 0.f};
#pragma unroll
  for (int ks = 0; ks < N / 8; ++ks) {
#pragma unroll
    for (int e = 0; e < 8; ++e) acc[e] = mfma(A[ks], tile_frag(R, ks, e), acc[e]);
    SCHED_FENCE();
  }
}
__device__ __forceinline__ bool tile64_partial(const f32x4 (&acc)[8], LAS unsigned* part, LAS unsigned* counter, int lane) {
  const int gh = lane >> 5, g1 = (lane >> 4) & 1, ch = (lane >> 3) & 1, c7 = lane & 7;
  float y[2][8];
#pragma unroll
  for (int e = 0; e < 8; ++e)
#pragma unroll
    for (int r = 0; r < 2; ++r) {
      auto s = __builtin_amdgcn_permlane32_swap(__float_as_uint(acc[e][r]), __float_as_uint(acc[e][r + 2]), false, false);
      const float own = gh ? __uint_as_float(s[1]) : __uint_as_float(s[0]), other = gh ? __uint_as_float(s[0]) : __uint_as_float(s[1]);
      y[r][e] = own + dpp<XOR8>(other);
    }
  if (gh == ch) {
#pragma unroll
    for (int r = 0; r < 2; ++r) {
      LAS unsigned* d = part + (4 * g1 + 2 * gh + r) * 64 + c7 * 8;
      *(LAS f32x4*)d = f32x4{y[r][0], y[r][1], y[r][2], y[r][3]};
      *(LAS f32x4*)(d + 4) = f32x4{y[r][4], y[r][5], y[r][6], y[r][7]};
    }
  }
  LDS_DONE();
  unsigned arrived = 0;
  if (lane == 0) arrived = __hip_atomic_fetch_add(counter, 1u, RLX_WG);
  const bool last = __builtin_amdgcn_readfirstlane(arrived) == 3u;
  FENCE();
  return last;
}
__device__ __forceinline__ f32x2 tile64_sum(LAS unsigned* part, int i, int lane) {
  f32x2 s = {0.f, 0.f};
#pragma unroll
  for (int w = 0; w < 4; ++w) s += *(LAS f32x2*)(part + w * 512 + i * 128 + lane * 2);
  return s;
}

struct Args {
  const unsigned short *x, *kc, *vc, *wq, *wo, *wg, *wu, *wd;
  unsigned short* out;
  unsigned char* ws;
};

#define STAMP(i) do {} while (0)
#define STAMP_FLUSH() do {} while (0)

__device__ __forceinline__ float div_by(float a, float d, float rd) {
  const float q = a * rd;
  return fmaf(fmaf(-q, d, a), rd, q);
}
__device__ __forceinline__ void ln_row(const u32x4& x0, const u32x4& x1, LAS unsigned* dst, int lane) {
  float v[16];
#pragma unroll
  for (int k = 0; k < 4; ++k) { v[2 * k] = bf_lo(x0[k]); v[2 * k + 1] = bf_hi(x0[k]); v[8 + 2 * k] = bf_lo(x1[k]); v[8 + 2 * k + 1] = bf_hi(x1[k]); }
  float s1 = 0.f;
#pragma unroll
  for (int k = 0; k < 8; ++k) s1 += v[k] + v[8 + k];
  const float mean = xrow_sum(row16_sum(s1)) * (1.f / D);
  float s2 = 0.f;
#pragma unroll
  for (int k = 0; k < 16; ++k) s2 = fmaf(v[k] - mean, v[k] - mean, s2);
  const float var = xrow_sum(row16_sum(s2)) * (1.f / D);
  const float m = rbf(mean), den = rbf(sqrtf(rbf(rbf(var) + 1e-5f))), rden = __builtin_amdgcn_rcpf(den);
  u32x4 o0, o1;
#pragma unroll
  for (int k = 0; k < 4; ++k) {
    const unsigned d0 = pk_bf16(v[2 * k] - m, v[2 * k + 1] - m), d1 = pk_bf16(v[8 + 2 * k] - m, v[8 + 2 * k + 1] - m);
    o0[k] = pk_bf16(div_by(bf_lo(d0), den, rden), div_by(bf_hi(d0), den, rden));
    o1[k] = pk_bf16(div_by(bf_lo(d1), den, rden), div_by(bf_hi(d1), den, rden));
  }
  *(LAS u32x4*)(dst + lane * 4) = o0;
  *(LAS u32x4*)(dst + 256 + lane * 4) = o1;
}

__global__ void __launch_bounds__(512, 2) fused_decode(Args a) {
  __shared__ __attribute__((aligned(16))) unsigned lds_[L_END + LDS_PAD];
  LAS unsigned* lds = (LAS unsigned*)lds_;
  u64 pc; unsigned len_a, len_b;
  asm volatile("s_getpc_b64 %0\n.Lfd_from:\n\ts_mov_b32 %1, .Lfd_mark_a-.Lfd_from\n\ts_mov_b32 %2, .Lfd_mark_b-.Lfd_from" : "=s"(pc), "=s"(len_a), "=s"(len_b));
  const unsigned code_len = len_a > len_b ? len_a : len_b;
  asm volatile("" :: "s"(a.x), "s"(a.kc), "s"(a.vc), "s"(a.wq), "s"(a.wo), "s"(a.wg), "s"(a.wu), "s"(a.wd), "s"(a.out), "s"(a.ws));
  const int lane = threadIdx.x & 63, wave = __builtin_amdgcn_readfirstlane(threadIdx.x >> 6);
  const int g = lane >> 4, c = lane & 15, gh = g >> 1, g1 = g & 1, c7 = c & 7, ch = c >> 3;
  const int wg = blockIdx.x, b = wg >> 5, h = wg & 31;
  unsigned stampv = 0; (void)stampv;
  const rsrc_t ws = mk_rsrc(a.ws, WS_END);
  u32x2 ep;

  if (wave == 0) STAMP(0);
  const u64 t_entry = now();
  const bool t_ffn = h < 14, t_q = h >= 16;
  const int u = wave - NS;
  const int tb = (b + TROT) & 7;
  const int tix = t_ffn ? h * 8 + tb : (h - 16) * 8 + tb;
  const int ld2 = (t_ffn ? F : H * DH) * 2;
  const int k0 = t_ffn ? 256 * (u & 3) : 512 * ((tix >> 6) & 1) + 128 * (u & 3);
  const rsrc_t rw1 = mk_rsrc((const char*)(t_ffn ? (tix < NFT ? a.wg : a.wu) : a.wq) + (size_t)k0 * ld2 + (t_ffn ? (tix < NFT ? tix : tix - NFT) : (tix & 63)) * 128,
                             (wave >= NS && (t_ffn | t_q) && !(ABL & 1)) ? 256 * ld2 : 0);
  const int voff1 = (32 * ch + 8 * g) * ld2 + c7 * 16;
  u32x4 R0[8];
  {
    const rsrc_t rx = mk_rsrc(a.x, B * D * 2);
    if (wave >= NS) {
#pragma unroll
      for (int i = 0; i < 8; ++i) R0[i] = ld16<W_AUX>(rw1, voff1, i * ld2);
    } else {
#pragma unroll
      for (int i = 0; i < 4; ++i) R0[i] = ld16<0>(rx, wave * 2048 + lane * 16, (i & 1) * 1024 + (i >> 1) * 8192);
      R0[4] = R0[5] = R0[6] = R0[7] = u32x4{0u, 0u, 0u, 0u};
      R0[4] = ld16<0>(mk_rsrc((const void*)pc, wave == 3 ? code_len : 0u), lane * 16 + (wg >> 3) * 1024, 0);
      const unsigned n = wave == 2 ? 16u : 0u;
      R0[5][0] = __builtin_amdgcn_raw_buffer_load_b32(mk_rsrc(a.kc + (size_t)wg * SK * DH, n), 0, 0, AUX_NT);
      R0[5][1] = __builtin_amdgcn_raw_buffer_load_b32(mk_rsrc(a.vc + (size_t)wg * SK * DH, n), 0, 0, AUX_NT);
      R0[5][2] = __builtin_amdgcn_raw_buffer_load_b32(mk_rsrc(a.wo + (size_t)h * 128 * D + b * 128, n), 0, 0, AUX_NT);
      R0[5][3] = __builtin_amdgcn_raw_buffer_load_b32(mk_rsrc(a.wd + (size_t)(h & 15) * 2 * FS * D + b * 128, n), 0, 0, AUX_NT);
      R0[6][0] = __builtin_amdgcn_raw_buffer_load_b32(mk_rsrc(a.ws + WS_Q + wg * 2048, n), 0, 0, AUX_NT);
      R0[6][1] = __builtin_amdgcn_raw_buffer_load_b32(mk_rsrc(a.ws + WS_P + wg * 8192, n), 0, 0, AUX_NT);
      R0[6][2] = __builtin_amdgcn_raw_buffer_load_b32(mk_rsrc(a.out, n), 0, 0, AUX_NT);
    }
    ep = __builtin_amdgcn_raw_buffer_load_b64(ws, WS_EPOCH, 0, 0);
    FENCE();
    if (wave == 0) { if (lane < 16) lds[L_FX + lane] = 0u; LDS_DONE(); }
    __builtin_amdgcn_s_barrier();
    FENCE();
  }

  if (wave < NS) {
    const rsrc_t rk = mk_rsrc(a.kc + (size_t)wg * SK * DH, SK * DH * 2);
    const rsrc_t rv = mk_rsrc(a.vc + (size_t)wg * SK * DH, SK * DH * 2);
    const int kvoff = lane * 16 + wave * 8192;
    if (t_ffn | t_q) {
#pragma unroll
      for (int i = 0; i < 2; ++i) ln_row(R0[2 * i], R0[2 * i + 1], lds + L_XN + (wave + 4 * i) * XN_STRIDE, lane);
      LDS_DONE();
      if (lane == 0) __hip_atomic_fetch_add(lds + L_FX, 1u, RLX_WG);
      FENCE();
    }
    asm volatile("" :: "v"(R0[4]), "v"(R0[5]), "v"(R0[6]));
    if ((t_ffn | t_q) && !(ABL & 1)) {
      const u64 limit = t_q ? SPIN_LIMIT : KV_T0;
      while (__hip_atomic_load(lds + L_FP, RLX_WG) != 4u && now() - t_entry < limit) __builtin_amdgcn_s_sleep(2);
      FENCE();
    }
    u32x4 R[8];
#pragma unroll
    for (int i = 0; i < 8; ++i) R[i] = ld16<AUX_NT>(rk, kvoff + (i & 3) * 1024, (i >> 2) * 4096);
    FENCE();
    if (ABL & 1) {
    } else if (wave == 0) {
      const Tag tag = mk_tag(epoch_hash(ep));
      u32x4 v[2];
      gather(v, ws, WS_Q + wg * 2048 + lane * 16, true, tag, [](int i) { return i * 1024; });
      lds[L_Q + lane] = pk_bf16(__uint_as_float(v[0][0]) + __uint_as_float(v[1][0]), __uint_as_float(v[0][2]) + __uint_as_float(v[1][2]));
      LDS_DONE();
      if (lane == 0) __hip_atomic_store(lds + L_FQ, 1u, RLX_WG);
      FENCE();
      STAMP(3);
      if (DBGOUT == 2 && lane == 0) lds[L_DBG] = (unsigned)now();
    } else {
      lds_wait(lds + L_FQ, 1u);
    }
    const u32x4 qv = *(LAS u32x4*)(lds + L_Q + c * 4);
    constexpr float SCALE = 0.08838834764831845f, LOG2E = 1.4426950408889634f;
    LAS float* sc = (LAS float*)(lds + L_S + wave * NSTEP * 64 + lane);
    float m = -INFINITY;
    for (int st = 0; st < NSTEP; ++st) {
      const bool last = st == NSTEP - 1;
      const rsrc_t rn = last ? rv : rk;
      const int so = last ? 0 : (st + 1) * 32768;
      float x[8];
#pragma unroll
      for (int i = 0; i < 8; ++i) {
        float d = 0.f;
#pragma unroll
        for (int k = 0; k < 4; ++k) d = dot2(R[i][k], qv[k], d);
        asm volatile("" : "+v"(d));
        x[i] = d;
        SCHED_FENCE();
        vm_wait<WIN_KV - 1>();
        R[i] = ld16<K_AUX>(rn, kvoff + (i & 3) * 1024, so + (i >> 2) * 4096);
        FENCE();
        SCHED_FENCE();
      }
#pragma unroll
      for (int j = 0; j < 4; ++j) { const float t1 = x[j] + dpp<XOR8>(x[j]), t2 = x[j + 4] + dpp<XOR8>(x[j + 4]); x[j] = (c & 8) ? t2 : t1; }
#pragma unroll
      for (int j = 0; j < 2; ++j) { const float t1 = x[j] + dpp<XOR7>(x[j]), t2 = x[j + 2] + dpp<XOR7>(x[j + 2]); x[j] = (c & 4) ? t2 : t1; }
      { const float t1 = x[0] + dpp<XOR2>(x[0]), t2 = x[1] + dpp<XOR2>(x[1]); x[0] = (c & 2) ? t2 : t1; }
      const float s = rbf(rbf(x[0] + dpp<XOR1>(x[0])) * SCALE) * LOG2E;
      m = fmaxf(m, s);
      sc[st * 64] = s;
    }
    if (wave == 0) STAMP(2);
    if (DBGOUT == 3 && wave == 0 && lane == 0) lds[L_DBG] = (unsigned)now();
    m = fmaxf(m, dpp<XOR2>(m)); m = fmaxf(m, dpp<XOR7>(m)); m = fmaxf(m, dpp<XOR8>(m));
    const float M = xrow_max(m);
    float l = 0.f, sn = sc[0];
    f32x2 acc[4];
#pragma unroll
    for (int k = 0; k < 4; ++k) acc[k] = f32x2{0.f, 0.f};
    for (int st = 0; st < NSTEP; ++st) {
      const int so = st == NSTEP - 1 ? 0x40000000 : (st + 1) * 32768;
      if (st == NSTEP - 3 && wave == 0 && lane == 0) __hip_atomic_store(lds + L_FE, 1u, RLX_WG);
      const float p = __builtin_amdgcn_exp2f(sn - M);
      l += p;
      sn = sc[(st + 1 < NSTEP ? st + 1 : st) * 64];
#define PV_STEP(i) { const float pb = dpp<0x150 + 2 * (i)>(p); const f32x2 p2 = {pb, pb};                                         \
        _Pragma("unroll") for (int k = 0; k < 4; ++k) {                                                                            \
          acc[k] = __builtin_elementwise_fma(p2, f32x2{bf_lo(R[i][k]), bf_hi(R[i][k])}, acc[k]); asm volatile("" : "+v"(acc[k])); } \
        SCHED_FENCE(); vm_wait<WIN_KV - 1>();                                                                                      \
        R[i] = ld16<V_AUX>(rv, kvoff + ((i) & 3) * 1024, so + ((i) >> 2) * 4096); FENCE(); SCHED_FENCE(); }
      PV_STEP(0) PV_STEP(1) PV_STEP(2) PV_STEP(3) PV_STEP(4) PV_STEP(5) PV_STEP(6) PV_STEP(7)
#undef PV_STEP
    }
    STAMP(4 + wave);
    l = xrow_sum(row16_sum(l)) * 0.5f;
#pragma unroll
    for (int k = 0; k < 4; ++k) acc[k] = f32x2{xrow_sum(acc[k][0]), xrow_sum(acc[k][1])};
    LAS unsigned* rec = lds + L_REC + wave * REC_STRIDE;
    if (g == 0) {
      *(LAS f32x4*)(rec + 8 * c) = f32x4{acc[0][0], acc[0][1], acc[1][0], acc[1][1]};
      *(LAS f32x4*)(rec + 8 * c + 4) = f32x4{acc[2][0], acc[2][1], acc[3][0], acc[3][1]};
      if (c == 0) *(LAS f32x2*)(rec + 128) = f32x2{M, l};
    }
    LDS_DONE();
    if (lane == 0) __hip_atomic_fetch_add(lds + L_FS, 1u, RLX_WG);
    STAMP_FLUSH();
    asm volatile(".Lfd_mark_a:");
    return;
  }

  if (ABL & 1) return;
  if (t_q) {
    u32x4 R[16];
#pragma unroll
    for (int i = 0; i < 8; ++i) { R[i] = R0[i]; vm_wait<WIN_Q - 1>(); R[8 + i] = ld16<W_AUX>(rw1, voff1, (64 + i) * ld2); FENCE(); }
    lds_wait(lds + L_FX, (unsigned)NS);
    u32x4 A[2]; f32x4 acc[8];
#pragma unroll
    for (int ks = 0; ks < 2; ++ks) A[ks] = *(LAS u32x4*)(lds + L_XN + c7 * XN_STRIDE + (k0 >> 1) + ks * 32 + ch * 16 + g * 4);
    tile_tmfma(acc, A, R);
    if (tile64_partial(acc, lds + L_P1 + u * 512, lds + L_FP, lane)) {
      const Tag tag = mk_tag(epoch_hash(ep));
#pragma unroll
      for (int i = 0; i < 4; ++i) {
        const f32x2 s = tile64_sum(lds + L_P1, i, lane);
        st16_sc1(piece(s[0], s[1], tag), ws,
                 WS_Q + (gh * 32 + ((tix & 63) >> 1)) * 2048 + (tix >> 6) * 1024 + (tix & 1) * 512 + (lane & 31) * 16, i * 2 * 32 * 2048);
      }
      FENCE();
      STAMP(9);
    }
  } else if (t_ffn) {
    u32x4 R[32];
#pragma unroll
    for (int i = 0; i < 8; ++i) R[i] = R0[i];
#pragma unroll
    for (int i = 8; i < 32; ++i) { vm_wait<WIN_F - 1>(); R[i] = ld16<W_AUX>(rw1, voff1, ((i >> 3) * 64 + (i & 7)) * ld2); FENCE(); }
    if (wave == 4) STAMP(34);
    lds_wait(lds + L_FX, (unsigned)NS);
    u32x4 A[4]; f32x4 acc[8];
#pragma unroll
    for (int ks = 0; ks < 4; ++ks) A[ks] = *(LAS u32x4*)(lds + L_XN + c7 * XN_STRIDE + (k0 >> 1) + ks * 32 + ch * 16 + g * 4);
    tile_tmfma(acc, A, R);
    if (tile64_partial(acc, lds + L_P1 + u * 512, lds + L_FP, lane)) {
      const Tag tag = mk_tag(epoch_hash(ep));
#pragma unroll
      for (int i = 0; i < 2; ++i) {
        f32x4 s = {0.f, 0.f, 0.f, 0.f};
#pragma unroll
        for (int w = 0; w < 4; ++w) s += *(LAS f32x4*)(lds + L_P1 + w * 512 + i * 256 + lane * 4);
        st16_sc1(piece(pk_bf16(s[0], s[1]), pk_bf16(s[2], s[3]), tag), ws,
                 (tix < NFT ? WS_G + tix * 256 : WS_U + (tix - NFT) * 256) + (g * F + c * 4) * 4, i * 4 * F * 4);
      }
      FENCE();
      STAMP(10);
    }
  }
  if (wave == 7 || (wave == 6 && !t_q)) { STAMP_FLUSH(); return; }

  u32x4 W[16];
  f32x4 acc[4];
  const int voff = (8 * g * D + 8 * c) * 2;
  u64 z;
  if (wave >= 5) {
    f32x4 a8[8];
    z = 0;
    if (t_q) {
      const int f0 = (2 * (h - 16) + (wave - 5)) * FS;
      LAS unsigned* ai = lds + L_AI + (wave - 5) * 8 * AI_STRIDE;
      u32x4 R[32];
      const rsrc_t rw = mk_rsrc(a.wd + (size_t)f0 * D + b * 128, FS * D * 2);
      tile_load<32, WIN_S>(R, rw, voff, g >= 2 ? 0x7ffffff0 : voff, 32 * D * 2, D * 2);
      if (wave == 5) STAMP(19);
      const Tag tag = mk_tag(epoch_hash(ep));
      u32x4 v[8];
      gather(v, ws, WS_G + (gh * F + f0 + (lane & 31) * 4) * 4, (lane & 31) < FS / 4, tag, [](int i) { return i * 2 * F * 4; });
      if (wave == 5) STAMP(21);
#pragma unroll
      for (int i = 0; i < 4; ++i) {
        const u32x4 gt = v[i], up = v[4 + i];
        *(LAS u32x2*)(ai + (2 * i + gh) * AI_STRIDE + (lane & 31) * 2) =
            u32x2{pk_bf16(bf_lo(gt[0]) * bf_lo(up[0]), bf_hi(gt[0]) * bf_hi(up[0])), pk_bf16(bf_lo(gt[2]) * bf_lo(up[2]), bf_hi(gt[2]) * bf_hi(up[2]))};
      }
      LDS_DONE();
      u32x4 A[4];
#pragma unroll
      for (int ks = 0; ks < 4; ++ks) A[ks] = *(LAS u32x4*)(ai + c7 * AI_STRIDE + ks * 16 + g * 4);
      tile_tmfma(a8, A, R);
      if (wave == 6) {
#pragma unroll
        for (int e = 0; e < 8; ++e) *(LAS f32x4*)(lds + L_A6 + e * 256 + lane * 4) = a8[e];
        LDS_DONE();
        if (lane == 0) __hip_atomic_store(lds + L_F6, 1u, RLX_WG);
        STAMP_FLUSH();
        return;
      }
      lds_wait(lds + L_F6, 1u);
#pragma unroll
      for (int e = 0; e < 8; ++e) a8[e] += *(LAS f32x4*)(lds + L_A6 + e * 256 + lane * 4);
    } else {
#pragma unroll
      for (int e = 0; e < 8; ++e) a8[e] = f32x4{0.f, 0.f, 0.f, 0.f};
    }
    z = epoch_hash(ep);
#pragma unroll
    for (int e = 0; e < 4; ++e) { *(LAS f32x4*)(lds + L_ACC + e * 256 + lane * 4) = a8[e]; acc[e] = a8[4 + e]; }
    LDS_DONE();
    if (lane == 0) __hip_atomic_store(lds + L_FA, 1u, RLX_WG);
    lds_wait(lds + L_FW, 1u);
#pragma unroll
    for (int i = 0; i < 16; ++i) { W[i] = *(LAS u32x4*)(lds + L_WO + i * 256 + lane * 4); asm volatile("" : "+v"(W[i])); }
    STAMP(22);
    lds_wait<LSLEEP>(lds + L_FE, 1u);
    __builtin_amdgcn_s_setprio(2);
    lds_wait(lds + L_FO, 1u);
  } else {
    {
      u32x4 R[32];
      tile_load<32, WIN_S>(R, mk_rsrc(a.wo + (size_t)h * 128 * D + b * 128, 128 * D * 2), voff, voff, 32 * D * 2, D * 2);
#pragma unroll
      for (int ks = 0; ks < 4; ++ks)
#pragma unroll
        for (int e = 0; e < 4; ++e) {
          W[ks * 4 + e] = tile_frag(R, ks, e);
          asm volatile("" : "+v"(W[ks * 4 + e]));
          *(LAS u32x4*)(lds + L_WO + (ks * 4 + e) * 256 + lane * 4) = tile_frag(R, ks, 4 + e);
        }
      LDS_DONE();
      if (lane == 0) __hip_atomic_store(lds + L_FW, 1u, RLX_WG);
    }
    z = epoch_hash(ep);
    const Tag tag = mk_tag(z);
    STAMP(13);
    lds_wait(lds + L_FA, 1u);
#pragma unroll
    for (int e = 0; e < 4; ++e) { acc[e] = *(LAS f32x4*)(lds + L_ACC + e * 256 + lane * 4); asm volatile("" : "+v"(acc[e])); }
    lds_wait<LSLEEP>(lds + L_FE, 1u);
    __builtin_amdgcn_s_setprio(2);
    lds_wait(lds + L_FS, (unsigned)NS);
    STAMP(14);
    if (DBGOUT == 4 && lane == 0) lds[L_DBG] = (unsigned)now();
    float mw[NS], lw[NS]; f32x2 aw[NS];
#pragma unroll
    for (int w = 0; w < NS; ++w) {
      const f32x2 ml = *(LAS f32x2*)(lds + L_REC + w * REC_STRIDE + 128);
      mw[w] = ml[0]; lw[w] = ml[1];
      aw[w] = *(LAS f32x2*)(lds + L_REC + w * REC_STRIDE + 2 * lane);
    }
    float M = mw[0];
#pragma unroll
    for (int w = 1; w < NS; ++w) M = fmaxf(M, mw[w]);
    float L = 0.f, o0 = 0.f, o1 = 0.f;
#pragma unroll
    for (int w = 0; w < NS; ++w) { const float f = __builtin_amdgcn_exp2f(mw[w] - M); L = fmaf(f, lw[w], L); o0 = fmaf(f, aw[w][0], o0); o1 = fmaf(f, aw[w][1], o1); }
    const float inv = __builtin_amdgcn_rcpf(L);
    const unsigned op = pk_bf16(div_by(o0, L, inv), div_by(o1, L, inv)), on = dppu<XOR1>(op);
    if ((lane & 1) == 0) st16_sc1(piece(op, on, tag), ws, WS_O + (h * 8 + b) * 512 + lane * 8, 0);
    STAMP(15);
    u32x4 v[4];
    gather(v, ws, WS_O + h * 4096 + lane * 16, true, tag, [](int i) { return i * 1024; });
    STAMP(16);
#pragma unroll
    for (int i = 0; i < 4; ++i) *(LAS u32x2*)(lds + L_AI + (2 * i + gh) * AI_STRIDE + (lane & 31) * 2) = u32x2{v[i][0], v[i][2]};
    LDS_DONE();
    if (lane == 0) __hip_atomic_store(lds + L_FO, 1u, RLX_WG);
    FENCE();
  }
  const Tag tag = mk_tag(z);
#pragma unroll
  for (int ks = 0; ks < 4; ++ks) {
    const u32x4 A = *(LAS u32x4*)(lds + L_AI + c7 * AI_STRIDE + ks * 16 + g * 4);
#pragma unroll
    for (int e = 0; e < 4; ++e) acc[e] = mfma(A, W[ks * 4 + e], acc[e]);
  }
#pragma unroll
  for (int r = 0; r < 4; ++r) {
    const float v0 = gh ? acc[2][r] : acc[0][r], v1 = gh ? acc[3][r] : acc[1][r];
    st16_sc1(piece(v0, v1, tag), ws,
             WS_P + ((b * 32 + 4 * g1 * 4 + (c >> 2)) * 32 + h) * 256 + u * 128 + ((c & 3) * 2 + gh) * 16, r * 4 * 32 * 256);
  }
  if (wave == 5) { STAMP_FLUSH(); return; }
  STAMP(17);
  {
    const int col = b * 128 + (h & 3) * 32 + (c7 >> 1) * 8 + ch * 4 + (c & 1) * 2;
    const unsigned xres = *(const GAS unsigned*)(a.x + (h >> 2) * D + col);
    u32x4 p[8];
    gather(p, ws, WS_P + (b * 32 + h) * 8192 + lane * 16, true, tag, [](int i) { return i * 1024; });
    float y0 = 0.f, y1 = 0.f;
#pragma unroll
    for (int i = 0; i < 8; ++i) { y0 += __uint_as_float(p[i][0]); y1 += __uint_as_float(p[i][2]); }
    y0 = xrow_sum(y0); y1 = xrow_sum(y1);
    if (g == 0) *(GAS unsigned*)(a.out + (h >> 2) * D + col) = pk_bf16(bf_lo(xres) + y0, bf_hi(xres) + y1);
  }
  if (wg == 0 && lane == 0) st8_sc1(u32x2{(unsigned)z, (unsigned)(z >> 32)}, ws, WS_EPOCH, 0);
  STAMP(18);
  STAMP_FLUSH();
  asm volatile(".Lfd_mark_b:");
}

}

extern "C" void kernel_launch(void* const* d_in, const int* in_sizes, int n_in, void* d_out, int out_size, void* d_ws, size_t ws_size,
                              hipStream_t stream) {
  Args a;
  a.x = (const unsigned short*)d_in[0];
  a.kc = (const unsigned short*)d_in[1];
  a.vc = (const unsigned short*)d_in[2];
  a.wq = (const unsigned short*)d_in[3];
  a.wo = (const unsigned short*)d_in[4];
  a.wg = (const unsigned short*)d_in[6];
  a.wu = (const unsigned short*)d_in[7];
  a.wd = (const unsigned short*)d_in[8];
  a.out = (unsigned short*)d_out;
  a.ws = (unsigned char*)d_ws;
  fused_decode<<<B * H, 512, 0, stream>>>(a);
}
```

```cpp
#include <hip/hip_runtime.h>
#include <hip/hip_bf16.h>
#include <stdint.h>

#define STAMPS 0
#define KV_T 4
#define POLL_SLEEP 2
#define WIN_Q 8
#define WIN_F 8
#define WIN_S 10
#define PRE 0
#define NX 0
#define WIN_K 8
#define WIN_KV 5
#define ABL 0
#define TROT 0
#define KV_T0 420
#define WMASK 0
#define WIN_KK(i) (WIN_KV + ((WMASK >> (i)) & 1))
#define WIN_VV(i) (WIN_KV + ((WMASK >> (4 + (i))) & 1))
#define TOUCH 1
#define SKEW 0
#define K_AUX 2
#define V_AUX 2
#define W_AUX 2
#define LSLEEP 16
#define DBGOUT 0
#define DBGWG 0
#define DBGSUB 0.f
#define PACE 1
#define PACE_FROM 8
#define PACE_T0 700
#define PACE_TAU 1716
#define PACE_BAND 0
#define W_TOP 7
#define PACE_A -50
#define PACE_B 100
#define PACE_C 250
#define XMAP 0
#define NPRE 8
#define KV_T1 0
#define ADAPT 1
#ifndef M_A
#define M_A 12
#endif
#ifndef M_B
#define M_B 24
#endif
#ifndef M_K
#define M_K 0.99f
#endif
#ifndef M_OFF
#define M_OFF 0
#endif
#ifndef M_WG
#define M_WG 46
#endif
#define OUT_SC1 0
#define D_O 16
#define D_P 32
#define S_O 0
#define S_P 0
#define CODE_PF 1

namespace {

typedef unsigned u32x4 __attribute__((ext_vector_type(4)));
typedef unsigned u32x2 __attribute__((ext_vector_type(2)));
typedef float f32x4 __attribute__((ext_vector_type(4)));
typedef float f32x2 __attribute__((ext_vector_type(2)));
typedef __bf16 bf16x2 __attribute__((ext_vector_type(2)));
typedef __bf16 bf16x8 __attribute__((ext_vector_type(8)));
typedef unsigned long long u64;
#define GAS __attribute__((address_space(1)))
#define LAS __attribute__((address_space(3)))
#define RLX_WG __ATOMIC_RELAXED, __HIP_MEMORY_SCOPE_WORKGROUP
typedef __amdgpu_buffer_rsrc_t rsrc_t;

constexpr int B = 8, D = 1024, H = 32, SK = 4096, DH = 128, F = 3584;
constexpr int FS = F / 32;
constexpr int NQT = H * DH / 64, NFT = F / 64;
constexpr int AUX_NT = 2, AUX_SC1 = 16;
constexpr int NS = 4;
constexpr int NSTEP = SK / (NS * 32);

constexpr int WS_EPOCH = 0;
constexpr int WS_Q = 4096;
constexpr int WS_G = WS_Q + B * H * 2 * DH * 8;
constexpr int WS_U = WS_G + B * F * 4;
constexpr int WS_O = WS_U + B * F * 4;
constexpr int WS_P = WS_O + B * H * DH * 4;
constexpr int WS_M = WS_P + 256 * 4096;
constexpr int WS_C = WS_M + 256 * 16;
constexpr int WS_END = WS_C + 32 * 128;
constexpr int WS_STAMP = 16 << 20;

constexpr unsigned SPIN_COUNT = 1u << 19;
constexpr u64 SPIN_LIMIT = 2000000;

constexpr int XN_STRIDE = 516;
constexpr int L_XN = 0;
constexpr int L_P1 = L_XN + 8 * XN_STRIDE;
constexpr int L_A6 = L_P1 + 2048;
constexpr int L_ACC = L_A6 + 2048;
constexpr int L_WO = L_ACC + 1024;
constexpr int AI_STRIDE = 68;
constexpr int L_AI = L_WO + 4096;
constexpr int L_Q = L_AI + 16 * AI_STRIDE;
constexpr int L_S = L_Q + 64;
constexpr int L_REC = L_S + NS * NSTEP * 64;
constexpr int REC_STRIDE = 132;
constexpr int L_FX = L_REC + NS * REC_STRIDE;
constexpr int L_FP = L_FX + 1, L_FQ = L_FX + 2, L_FS = L_FX + 3, L_FA = L_FX + 4, L_FW = L_FX + 5, L_FO = L_FX + 6, L_F6 = L_FX + 7, L_FE = L_FX + 8, L_DBG = L_FX + 9, L_FM = L_FX + 10, L_M = L_FX + 12, L_CT = L_FX + 14;
constexpr int L_END = L_FX + 16;

__device__ __forceinline__ float bf_lo(unsigned u) { return __uint_as_float(u << 16); }
__device__ __forceinline__ float bf_hi(unsigned u) { return __uint_as_float(u & 0xffff0000u); }
__device__ __forceinline__ unsigned pk_bf16(float lo, float hi) {
  const f32x2 v = {lo, hi};
  return __builtin_bit_cast(unsigned, __builtin_convertvector(v, bf16x2));
}
__device__ __forceinline__ float rbf(float x) { return bf_lo(pk_bf16(x, x)); }
__device__ __forceinline__ float dot2(unsigned k, unsigned q, float acc) {
  return __builtin_amdgcn_fdot2_f32_bf16(__builtin_bit_cast(bf16x2, k), __builtin_bit_cast(bf16x2, q), acc, false);
}
template <int CTRL> __device__ __forceinline__ unsigned dppu(unsigned x) { return (unsigned)__builtin_amdgcn_mov_dpp((int)x, CTRL, 0xf, 0xf, true); }
template <int CTRL> __device__ __forceinline__ float dpp(float x) { return __uint_as_float(dppu<CTRL>(__float_as_uint(x))); }
constexpr int XOR1 = 0xB1, XOR2 = 0x4E, XOR7 = 0x141, XOR8 = 0x128;
__device__ __forceinline__ float row16_sum(float d) {
  d += dpp<XOR1>(d); d += dpp<XOR2>(d); d += dpp<XOR7>(d); d += dpp<XOR8>(d);
  return d;
}
__device__ __forceinline__ float xrow_sum(float x) {
  auto s = __builtin_amdgcn_permlane16_swap(__float_as_uint(x), __float_as_uint(x), false, false);
  x = __uint_as_float(s[0]) + __uint_as_float(s[1]);
  auto t = __builtin_amdgcn_permlane32_swap(__float_as_uint(x), __float_as_uint(x), false, false);
  return __uint_as_float(t[0]) + __uint_as_float(t[1]);
}
__device__ __forceinline__ float xrow_max(float x) {
  auto s = __builtin_amdgcn_permlane16_swap(__float_as_uint(x), __float_as_uint(x), false, false);
  x = fmaxf(__uint_as_float(s[0]), __uint_as_float(s[1]));
  auto t = __builtin_amdgcn_permlane32_swap(__float_as_uint(x), __float_as_uint(x), false, false);
  return fmaxf(__uint_as_float(t[0]), __uint_as_float(t[1]));
}
__device__ __forceinline__ rsrc_t mk_rsrc(const void* p, unsigned bytes) {
  return __builtin_amdgcn_make_buffer_rsrc(const_cast<void*>(p), 0, bytes, 0x00020000);
}
template <int AUX> __device__ __forceinline__ u32x4 ld16(rsrc_t r, int voff, int soff) { return __builtin_amdgcn_raw_buffer_load_b128(r, voff, soff, AUX); }
__device__ __forceinline__ void st16_sc1(u32x4 v, rsrc_t r, int voff, int off) { __builtin_amdgcn_raw_buffer_store_b128(v, r, voff + off, 0, AUX_SC1); }
__device__ __forceinline__ void st8_sc1(u32x2 v, rsrc_t r, int voff, int soff) { __builtin_amdgcn_raw_buffer_store_b64(v, r, voff, soff, AUX_SC1); }
#define FENCE() asm volatile("" ::: "memory")
#define LDS_DONE() asm volatile("s_waitcnt lgkmcnt(0)" ::: "memory")
#define SCHED_FENCE() __builtin_amdgcn_sched_barrier(0)
template <int N> __device__ __forceinline__ void vm_wait() { asm volatile("s_waitcnt vmcnt(%0)" ::"n"(N) : "memory"); }
__device__ __forceinline__ void vm_wait_n(int n) {
  switch (n) { case 3: vm_wait<3>(); break; case 4: vm_wait<4>(); break; case 5: vm_wait<5>(); break; case 6: vm_wait<6>(); break; default: vm_wait<7>(); }
}
__device__ __forceinline__ void window_wait(int mode) {
  asm volatile("s_cmp_lt_u32 %0, 2\n\ts_cbranch_scc1 .Lwa%=\n\ts_cmp_eq_u32 %0, 2\n\ts_cbranch_scc1 .Lwb%=\n\ts_waitcnt vmcnt(%4)\n.Lwb%=:\n\ts_waitcnt vmcnt(%3)\n"
               ".Lwa%=:\n\ts_cmp_eq_u32 %0, 0\n\ts_cbranch_scc1 .Lwc%=\n\ts_waitcnt vmcnt(%2)\n.Lwc%=:\n\ts_waitcnt vmcnt(%1)"
               :: "s"(mode), "n"(W_TOP - 1), "n"(W_TOP - 2), "n"(W_TOP - 3), "n"(W_TOP - 4) : "scc", "memory");
}
__device__ __forceinline__ u64 now() { return __builtin_amdgcn_s_memrealtime(); }

struct Tag { unsigned a, b; };
struct Tag12 { unsigned t[4]; };
__device__ __forceinline__ bool fresh(const u32x4& v, const Tag12& t) { return (((v[0] ^ t.t[0]) | (v[1] ^ t.t[1]) | (v[2] ^ t.t[2]) | (v[3] ^ t.t[3])) & 0xfffu) == 0u; }
__device__ __forceinline__ unsigned enc20(float x, unsigned t12) { const unsigned u = __float_as_uint(x); return ((u + 0x7ffu + ((u >> 12) & 1u)) & 0xfffff000u) | t12; }
__device__ __forceinline__ float dec20(unsigned v) { return __uint_as_float(v & 0xfffff000u); }
__device__ __forceinline__ u32x4 stale(bool act, const Tag12& t) { return u32x4{act ? ~t.t[0] : t.t[0], t.t[1], t.t[2], t.t[3]}; }
__device__ __forceinline__ bool fresh(const u32x4& v, const Tag& t) { return (v[1] == t.a) & (v[3] == t.b); }
__device__ __forceinline__ u32x4 piece(unsigned p0, unsigned p1, const Tag& t) { return u32x4{p0, t.a, p1, t.b}; }
__device__ __forceinline__ u32x4 piece(float p0, float p1, const Tag& t) { return u32x4{__float_as_uint(p0), t.a, __float_as_uint(p1), t.b}; }

__device__ __forceinline__ u32x4 stale(bool act, const Tag& t) { return u32x4{0u, act ? ~t.a : t.a, 0u, t.b}; }
template <int N, class TG> __device__ __forceinline__ void gather_init(u32x4 (&v)[N], bool act, const TG& t) {
#pragma unroll
  for (int i = 0; i < N; ++i) v[i] = stale(act, t);
}
template <int N, class TG, class Off> __device__ __forceinline__ bool gather_pass(u32x4 (&v)[N], rsrc_t ws, int voff, const TG& t, Off off) {
  FENCE();
#pragma unroll
  for (int i = 0; i < N; ++i)
    if (!fresh(v[i], t)) v[i] = ld16<AUX_SC1>(ws, voff, off(i));
  bool ok = true;
#pragma unroll
  for (int i = 0; i < N; ++i) ok &= fresh(v[i], t);
  const bool all = __all(ok);
  FENCE();
  return all;
}
template <int SLEEP, int N, class TG, class Off> __device__ __forceinline__ void gather_wait(u32x4 (&v)[N], rsrc_t ws, int voff, const TG& t, Off off) {
  const u64 t0 = now();
  while (!gather_pass(v, ws, voff, t, off)) {
    if (now() - t0 > SPIN_LIMIT) break;
    if (SLEEP) __builtin_amdgcn_s_sleep(SLEEP);
  }
}
template <int SLEEP = POLL_SLEEP, int N, class TG, class Off> __device__ __forceinline__ void gather(u32x4 (&v)[N], rsrc_t ws, int voff, bool act, const TG& t, Off off) {
  gather_init(v, act, t);
  gather_wait<SLEEP>(v, ws, voff, t, off);
}
__device__ __forceinline__ u64 epoch_hash(u32x2 e) {
  asm volatile("" : "+v"(e));
  u64 z = (((u64)(unsigned)__builtin_amdgcn_readfirstlane(e[1]) << 32) | (unsigned)__builtin_amdgcn_readfirstlane(e[0])) + 0x9E3779B97F4A7C15ull;
  z = (z ^ (z >> 30)) * 0xBF58476D1CE4E5B9ull; z = (z ^ (z >> 27)) * 0x94D049BB133111EBull;
  return z ^ (z >> 31);
}
__device__ __forceinline__ Tag12 mk_tag12(u64 z) { return Tag12{{(unsigned)z & 0xfffu, (unsigned)(z >> 12) & 0xfffu, (unsigned)(z >> 24) & 0xfffu, (unsigned)(z >> 36) & 0xfffu}}; }
__device__ __forceinline__ Tag mk_tag(u64 z) { return Tag{(unsigned)z, (unsigned)(z >> 32)}; }
template <int SLEEP = 1> __device__ __forceinline__ void lds_wait(LAS unsigned* w, unsigned want) {
  for (unsigned n = 0; __hip_atomic_load(w, RLX_WG) != want && n < SPIN_COUNT / SLEEP; ++n) __builtin_amdgcn_s_sleep(SLEEP);
  FENCE();
}

template <int N, int WIN, int NP> __device__ __forceinline__ void tile_load(u32x4 (&R)[N], const u32x4 (&P)[NP], rsrc_t r, int voff, int voff3, int step, int row) {
#pragma unroll
  for (int i = 0; i < NP; ++i) R[i] = P[i];
#pragma unroll
  for (int i = NP; i < N; ++i) {
    vm_wait<WIN - 1>();
    R[i] = ld16<W_AUX>(r, i < 24 ? voff : voff3, (i >> 3) * step + (i & 7) * row);
    FENCE();
  }
}
template <int N> __device__ __forceinline__ u32x4 tile_frag(const u32x4 (&R)[N], int ks, int e) {
  u32x4 f;
#pragma unroll
  for (int p = 0; p < 4; ++p)
    f[p] = __builtin_amdgcn_perm(R[ks * 8 + 2 * p + 1][e >> 1], R[ks * 8 + 2 * p][e >> 1], (e & 1) ? 0x07060302u : 0x05040100u);
  return f;
}
__device__ __forceinline__ f32x4 mfma(const u32x4& a, const u32x4& b, const f32x4& c) {
  return __builtin_amdgcn_mfma_f32_16x16x32_bf16(__builtin_bit_cast(bf16x8, a), __builtin_bit_cast(bf16x8, b), c, 0, 0, 0);
}
template <int N> __device__ __forceinline__ void tile_tmfma(f32x4 (&acc)[8], const u32x4 (&A)[N / 8], const u32x4 (&R)[N]) {
#pragma unroll
  for (int e = 0; e < 8; ++e) acc[e] = f32x4{0.f, 0.f, 0.f, 0.f};
#pragma unroll
  for (int ks = 0; ks < N / 8; ++ks) {
#pragma unroll
    for (int e = 0; e < 8; ++e) acc[e] = mfma(A[ks], tile_frag(R, ks, e), acc[e]);
    SCHED_FENCE();
  }
}
__device__ __forceinline__ bool tile64_partial(const f32x4 (&acc)[8], LAS unsigned* part, LAS unsigned* counter, int lane) {
  const int gh = lane >> 5, g1 = (lane >> 4) & 1, ch = (lane >> 3) & 1, c7 = lane & 7;
  float y[2][8];
#pragma unroll
  for (int e = 0; e < 8; ++e)
#pragma unroll
    for (int r = 0; r < 2; ++r) {
      auto s = __builtin_amdgcn_permlane32_swap(__float_as_uint(acc[e][r]), __float_as_uint(acc[e][r + 2]), false, false);
      const float own = gh ? __uint_as_float(s[1]) : __uint_as_float(s[0]), other = gh ? __uint_as_float(s[0]) : __uint_as_float(s[1]);
      y[r][e] = own + dpp<XOR8>(other);
    }
  if (gh == ch) {
#pragma unroll
    for (int r = 0; r < 2; ++r) {
      LAS unsigned* d = part + (4 * g1 + 2 * gh + r) * 64 + c7 * 8;
      *(LAS f32x4*)d = f32x4{y[r][0], y[r][1], y[r][2], y[r][3]};
      *(LAS f32x4*)(d + 4) = f32x4{y[r][4], y[r][5], y[r][6], y[r][7]};
    }
  }
  LDS_DONE();
  unsigned arrived = 0;
  if (lane == 0) arrived = __hip_atomic_fetch_add(counter, 1u, RLX_WG);
  const bool last = __builtin_amdgcn_readfirstlane(arrived) == 3u;
  FENCE();
  return last;
}
__device__ __forceinline__ f32x2 tile64_sum(LAS unsigned* part, int i, int lane) {
  f32x2 s = {0.f, 0.f};
#pragma unroll
  for (int w = 0; w < 4; ++w) s += *(LAS f32x2*)(part + w * 512 + i * 128 + lane * 2);
  return s;
}

struct Args {
  const unsigned short *x, *kc, *vc, *wq, *wo, *wg, *wu, *wd;
  unsigned short* out;
  unsigned char* ws;
};

#define STAMP(i) do {} while (0)
#define STAMP_FLUSH() do {} while (0)

__device__ __forceinline__ float div_by(float a, float d, float rd) {
  const float q = a * rd;
  return fmaf(fmaf(-q, d, a), rd, q);
}
__device__ __forceinline__ void ln_row(const u32x4& x0, const u32x4& x1, LAS unsigned* dst, int lane) {
  float v[16];
#pragma unroll
  for (int k = 0; k < 4; ++k) { v[2 * k] = bf_lo(x0[k]); v[2 * k + 1] = bf_hi(x0[k]); v[8 + 2 * k] = bf_lo(x1[k]); v[8 + 2 * k + 1] = bf_hi(x1[k]); }
  float s1 = 0.f;
#pragma unroll
  for (int k = 0; k < 8; ++k) s1 += v[k] + v[8 + k];
  const float mean = xrow_sum(row16_sum(s1)) * (1.f / D);
  float s2 = 0.f;
#pragma unroll
  for (int k = 0; k < 16; ++k) s2 = fmaf(v[k] - mean, v[k] - mean, s2);
  const float var = xrow_sum(row16_sum(s2)) * (1.f / D);
  const float m = rbf(mean), den = rbf(__builtin_amdgcn_sqrtf(rbf(rbf(var) + 1e-5f))), rden = __builtin_amdgcn_rcpf(den);
  u32x4 o0, o1;
#pragma unroll
  for (int k = 0; k < 4; ++k) {
    const unsigned d0 = pk_bf16(v[2 * k] - m, v[2 * k + 1] - m), d1 = pk_bf16(v[8 + 2 * k] - m, v[8 + 2 * k + 1] - m);
    o0[k] = pk_bf16(div_by(bf_lo(d0), den, rden), div_by(bf_hi(d0), den, rden));
    o1[k] = pk_bf16(div_by(bf_lo(d1), den, rden), div_by(bf_hi(d1), den, rden));
  }
  *(LAS u32x4*)(dst + lane * 4) = o0;
  *(LAS u32x4*)(dst + 256 + lane * 4) = o1;
}

__global__ void __launch_bounds__(512, 2) fused_decode(Args a) {
  __shared__ __attribute__((aligned(16))) unsigned lds_[L_END];
  LAS unsigned* lds = (LAS unsigned*)lds_;
  u64 pc; unsigned len_a, len_b;
  asm volatile("s_getpc_b64 %0\n.Lfd_from:\n\ts_mov_b32 %1, .Lfd_mark_a-.Lfd_from\n\ts_mov_b32 %2, .Lfd_mark_b-.Lfd_from" : "=s"(pc), "=s"(len_a), "=s"(len_b));
  const unsigned code_len = len_a > len_b ? len_a : len_b;
  asm volatile("" :: "s"(a.x), "s"(a.kc), "s"(a.vc), "s"(a.wq), "s"(a.wo), "s"(a.wg), "s"(a.wu), "s"(a.wd), "s"(a.out), "s"(a.ws));
  const int lane = threadIdx.x & 63, wave = __builtin_amdgcn_readfirstlane(threadIdx.x >> 6);
  const int g = lane >> 4, c = lane & 15, gh = g >> 1, g1 = g & 1, c7 = c & 7, ch = c >> 3;
  const int wg = XMAP ? ((blockIdx.x & 7) << 5) | (blockIdx.x >> 3) : blockIdx.x, b = wg >> 5, h = wg & 31;
  unsigned stampv = 0; (void)stampv;
  const rsrc_t ws = mk_rsrc(a.ws, WS_END);
  u32x2 ep;

  if (wave == 0) STAMP(0);
  const u64 t_entry = now();
  const bool t_ffn = h < 14, t_q = h >= 16;
  const int u = wave - NS;
  const int tb = (b + TROT) & 7;
  const int tix = t_ffn ? h * 8 + tb : (h - 16) * 8 + tb;
  const int ld2 = (t_ffn ? F : H * DH) * 2;
  const int k0 = t_ffn ? 256 * (u & 3) : 512 * ((tix >> 6) & 1) + 128 * (u & 3);
  const rsrc_t rw1 = mk_rsrc((const char*)(t_ffn ? (tix < NFT ? a.wg : a.wu) : a.wq) + (size_t)k0 * ld2 + (t_ffn ? (tix < NFT ? tix : tix - NFT) : (tix & 63)) * 128,
                             (wave >= NS && (t_ffn | t_q) && !(ABL & 1)) ? 256 * ld2 : 0);
  const int voff1 = (32 * ch + 8 * g) * ld2 + c7 * 16;
  u32x4 R0[8];
  {
    const rsrc_t rx = mk_rsrc(a.x, B * D * 2);
    if (wave >= NS) {
#pragma unroll
      for (int i = 0; i < 8; ++i) R0[i] = ld16<W_AUX>(rw1, voff1, i * ld2);
    } else {
#pragma unroll
      for (int i = 0; i < 4; ++i) R0[i] = ld16<0>(rx, wave * 2048 + lane * 16, (i & 1) * 1024 + (i >> 1) * 8192);
      R0[4] = R0[5] = R0[6] = R0[7] = u32x4{0u, 0u, 0u, 0u};
      R0[4] = ld16<0>(mk_rsrc((const void*)pc, wave == 3 ? code_len : 0u), lane * 16 + (blockIdx.x >> 3) * 1024, 0);
      const unsigned n = wave == 2 ? 16u : 0u;
      R0[5][0] = __builtin_amdgcn_raw_buffer_load_b32(mk_rsrc(a.kc + (size_t)wg * SK * DH, n), 0, 0, AUX_NT);
      R0[5][1] = __builtin_amdgcn_raw_buffer_load_b32(mk_rsrc(a.vc + (size_t)wg * SK * DH, n), 0, 0, AUX_NT);
      R0[5][2] = __builtin_amdgcn_raw_buffer_load_b32(mk_rsrc(a.wo + (size_t)h * 128 * D + b * 128, n), 0, 0, AUX_NT);
      R0[5][3] = __builtin_amdgcn_raw_buffer_load_b32(mk_rsrc(a.wd + (size_t)(h & 15) * 2 * FS * D + b * 128, n), 0, 0, AUX_NT);
      R0[6][0] = __builtin_amdgcn_raw_buffer_load_b32(mk_rsrc(a.ws + WS_Q + wg * 2048, n), 0, 0, AUX_NT);
      R0[6][1] = __builtin_amdgcn_raw_buffer_load_b32(mk_rsrc(a.ws + WS_P + wg * 4096, n), 0, 0, AUX_NT);
      R0[6][2] = __builtin_amdgcn_raw_buffer_load_b32(mk_rsrc(a.out, n), 0, 0, AUX_NT);
    }
    ep = __builtin_amdgcn_raw_buffer_load_b64(ws, WS_EPOCH, 0, 0);
    FENCE();
    if (wave == 0) { if (lane < 16) lds[L_FX + lane] = lane == L_CT - L_FX ? (unsigned)t_entry + PACE_T0 : (lane == L_CT + 1 - L_FX ? PACE_TAU : 0u); LDS_DONE(); }
    __builtin_amdgcn_s_barrier();
    FENCE();
  }

  if (wave < NS) {
    const rsrc_t rk = mk_rsrc(a.kc + (size_t)wg * SK * DH, SK * DH * 2);
    const rsrc_t rv = mk_rsrc(a.vc + (size_t)wg * SK * DH, SK * DH * 2);
    const int kvoff = lane * 16 + wave * 8192;
    if (t_ffn | t_q) {
#pragma unroll
      for (int i = 0; i < 2; ++i) ln_row(R0[2 * i], R0[2 * i + 1], lds + L_XN + (wave + 4 * i) * XN_STRIDE, lane);
      LDS_DONE();
      if (lane == 0) __hip_atomic_fetch_add(lds + L_FX, 1u, RLX_WG);
      FENCE();
    }
    asm volatile("" :: "v"(R0[4]), "v"(R0[5]), "v"(R0[6]));
    if ((t_ffn | t_q) && !(ABL & 1)) {
      const u64 limit = t_q ? SPIN_LIMIT : KV_T0;
      while (__hip_atomic_load(lds + L_FP, RLX_WG) != 5u && now() - t_entry < limit) __builtin_amdgcn_s_sleep(1);
      if (KV_T1) while (__hip_atomic_load(lds + L_FW, RLX_WG) != 1u && now() - t_entry < KV_T1) __builtin_amdgcn_s_sleep(1);
      FENCE();
    }
    u32x4 R[8];
#pragma unroll
    for (int i = 0; i < 8; ++i) R[i] = ld16<AUX_NT>(rk, kvoff + (i & 3) * 1024, (i >> 2) * 4096);
    FENCE();
    const Tag stag = mk_tag(epoch_hash(ep));
    if (ABL & 1) {
    } else if (wave == 0) {
      u32x4 v[2];
      gather(v, ws, WS_Q + wg * 2048 + lane * 16, true, stag, [](int i) { return i * 1024; });
      lds[L_Q + lane] = pk_bf16(__uint_as_float(v[0][0]) + __uint_as_float(v[1][0]), __uint_as_float(v[0][2]) + __uint_as_float(v[1][2]));
      LDS_DONE();
      if (lane == 0) __hip_atomic_store(lds + L_FQ, 1u, RLX_WG);
      FENCE();
      STAMP(3);
      if (DBGOUT == 2 && lane == 0) lds[L_DBG] = (unsigned)now();
    } else {
      lds_wait(lds + L_FQ, 1u);
    }
    const u32x4 qv = *(LAS u32x4*)(lds + L_Q + c * 4);
    constexpr float SCALE = 0.08838834764831845f, LOG2E = 1.4426950408889634f;
    LAS float* sc = (LAS float*)(lds + L_S + wave * NSTEP * 64 + lane);
    auto pace = [&](int step) {
      const unsigned t = (unsigned)now();
      const u32x2 ct = *(volatile LAS u32x2*)(lds + L_CT);
      if (ADAPT && wave == 0 && (step == M_A || step == M_B) && lane == 0) { lds[L_M + (step == M_B)] = t; if (step == M_B) __hip_atomic_store(lds + L_FM, 1u, RLX_WG); }
      if (PACE == 0 || step < PACE_FROM) return 2;
      const int late = (int)(t - (unsigned)__builtin_amdgcn_readfirstlane(ct[0]) - (((unsigned)step * (unsigned)__builtin_amdgcn_readfirstlane(ct[1])) >> 4));
      return late > PACE_C ? 0 : (late > PACE_B ? 1 : (late > PACE_A ? 2 : 3));
    };
    float m = -INFINITY;
    for (int st = 0; st < NSTEP; ++st) {
      const int mode = pace(st);
      const bool last = st == NSTEP - 1;
      const rsrc_t rn = last ? rv : rk;
      const int so = last ? 0 : (st + 1) * 32768;
      float x[8];
#pragma unroll
      for (int i = 0; i < 8; ++i) {
        float d = 0.f;
#pragma unroll
        for (int k = 0; k < 4; ++k) d = dot2(R[i][k], qv[k], d);
        asm volatile("" : "+v"(d));
        x[i] = d;
        SCHED_FENCE();
        window_wait(mode);
        R[i] = ld16<K_AUX>(rn, kvoff + (i & 3) * 1024, so + (i >> 2) * 4096);
        FENCE();
        SCHED_FENCE();
      }
#pragma unroll
      for (int j = 0; j < 4; ++j) { const float t1 = x[j] + dpp<XOR8>(x[j]), t2 = x[j + 4] + dpp<XOR8>(x[j + 4]); x[j] = (c & 8) ? t2 : t1; }
#pragma unroll
      for (int j = 0; j < 2; ++j) { const float t1 = x[j] + dpp<XOR7>(x[j]), t2 = x[j + 2] + dpp<XOR7>(x[j + 2]); x[j] = (c & 4) ? t2 : t1; }
      { const float t1 = x[0] + dpp<XOR2>(x[0]), t2 = x[1] + dpp<XOR2>(x[1]); x[0] = (c & 2) ? t2 : t1; }
      const float s = rbf(rbf(x[0] + dpp<XOR1>(x[0])) * SCALE) * LOG2E;
      m = fmaxf(m, s);
      sc[st * 64] = s;
    }
    if (wave == 0) STAMP(2);
    if (DBGOUT == 3 && wave == 0 && lane == 0) lds[L_DBG] = (unsigned)now();
    m = fmaxf(m, dpp<XOR2>(m)); m = fmaxf(m, dpp<XOR7>(m)); m = fmaxf(m, dpp<XOR8>(m));
    const float M = xrow_max(m);
    float l = 0.f, sn = sc[0];
    f32x2 acc[4];
#pragma unroll
    for (int k = 0; k < 4; ++k) acc[k] = f32x2{0.f, 0.f};
    for (int st = 0; st < NSTEP; ++st) {
      const int mode = pace(NSTEP + st);
      const int so = st == NSTEP - 1 ? 0x40000000 : (st + 1) * 32768;
      if (st == NSTEP - 3 && wave == 0 && lane == 0) __hip_atomic_store(lds + L_FE, 1u, RLX_WG);
      const float p = __builtin_amdgcn_exp2f(sn - M);
      l += p;
      sn = sc[(st + 1 < NSTEP ? st + 1 : st) * 64];
#define PV_STEP(i) { const float pb = dpp<0x150 + 2 * (i)>(p); const f32x2 p2 = {pb, pb};                                         \
        _Pragma("unroll") for (int k = 0; k < 4; ++k) {                                                                            \
          acc[k] = __builtin_elementwise_fma(p2, f32x2{bf_lo(R[i][k]), bf_hi(R[i][k])}, acc[k]); asm volatile("" : "+v"(acc[k])); } \
        SCHED_FENCE(); window_wait(mode);                                                                                          \
        R[i] = ld16<V_AUX>(rv, kvoff + ((i) & 3) * 1024, so + ((i) >> 2) * 4096); FENCE(); SCHED_FENCE(); }
      PV_STEP(0) PV_STEP(1) PV_STEP(2) PV_STEP(3) PV_STEP(4) PV_STEP(5) PV_STEP(6) PV_STEP(7)
#undef PV_STEP
    }
    STAMP(4 + wave);
    f32x2 y[2];
#pragma unroll
    for (int k = 0; k < 2; ++k)
#pragma unroll
      for (int j = 0; j < 2; ++j) {
        auto t = __builtin_amdgcn_permlane32_swap(__float_as_uint(acc[k][j]), __float_as_uint(acc[k + 2][j]), false, false);
        y[k][j] = __uint_as_float(t[0]) + __uint_as_float(t[1]);
      }
    f32x2 o2;
#pragma unroll
    for (int j = 0; j < 2; ++j) {
      auto t = __builtin_amdgcn_permlane16_swap(__float_as_uint(y[0][j]), __float_as_uint(y[1][j]), false, false);
      o2[j] = __uint_as_float(t[0]) + __uint_as_float(t[1]);
    }
    l = xrow_sum(row16_sum(l)) * 0.5f;
    LAS unsigned* rec = lds + L_REC + wave * REC_STRIDE;
    *(LAS f32x2*)(rec + 8 * c + 2 * g) = o2;
    if (lane == 0) *(LAS f32x2*)(rec + 128) = f32x2{M, l};
    LDS_DONE();
    unsigned arrived = 0;
    if (lane == 0) arrived = __hip_atomic_fetch_add(lds + L_FS, 1u, RLX_WG);
    if (__builtin_amdgcn_readfirstlane(arrived) == NS - 1) {
      FENCE();
      STAMP(14);
      float mw[NS], lw[NS]; f32x2 aw[NS];
#pragma unroll
      for (int w = 0; w < NS; ++w) {
        const f32x2 ml = *(LAS f32x2*)(lds + L_REC + w * REC_STRIDE + 128);
        mw[w] = ml[0]; lw[w] = ml[1];
        aw[w] = *(LAS f32x2*)(lds + L_REC + w * REC_STRIDE + 2 * lane);
      }
      const float MM = fmaxf(fmaxf(mw[0], mw[1]), fmaxf(mw[2], mw[3]));
      float L = 0.f, o0 = 0.f, o1 = 0.f;
#pragma unroll
      for (int w = 0; w < NS; ++w) { const float f = __builtin_amdgcn_exp2f(mw[w] - MM); L = fmaf(f, lw[w], L); o0 = fmaf(f, aw[w][0], o0); o1 = fmaf(f, aw[w][1], o1); }
      const float inv = __builtin_amdgcn_rcpf(L);
      const unsigned op = pk_bf16(div_by(o0, L, inv), div_by(o1, L, inv)), on = dppu<XOR1>(op);
      if ((lane & 1) == 0) st16_sc1(piece(op, on, stag), ws, WS_O + (h * 8 + b) * 512 + lane * 8, 0);
      STAMP(15);
    }
    STAMP_FLUSH();
    asm volatile(".Lfd_mark_a:");
    return;
  }

  if (ABL & 1) return;
  const int voff = (8 * g * D + 8 * c) * 2;
  const int f0 = (2 * (h - 16) + (wave - 5)) * FS;
  const rsrc_t rw2 = wave == 4 ? mk_rsrc(a.wo + (size_t)h * 128 * D + b * 128, 128 * D * 2)
                               : mk_rsrc(a.wd + (size_t)(t_q ? f0 : 0) * D + b * 128, (t_q && wave < 7) ? FS * D * 2 : 0);
  u32x4 N0[NPRE];
#define NEXT_FIRST() _Pragma("unroll") for (int i = 0; i < NPRE; ++i) { vm_wait<7>(); N0[i] = ld16<W_AUX>(rw2, voff, i * D * 2); FENCE(); }
  if (t_q) {
    u32x4 R[16];
#pragma unroll
    for (int i = 0; i < 8; ++i) { R[i] = R0[i]; vm_wait<WIN_Q - 1>(); R[8 + i] = ld16<W_AUX>(rw1, voff1, (64 + i) * ld2); FENCE(); }
    NEXT_FIRST();
    lds_wait(lds + L_FX, (unsigned)NS);
    u32x4 A[2]; f32x4 acc[8];
#pragma unroll
    for (int ks = 0; ks < 2; ++ks) A[ks] = *(LAS u32x4*)(lds + L_XN + c7 * XN_STRIDE + (k0 >> 1) + ks * 32 + ch * 16 + g * 4);
    if (wave == 4) STAMP(35);
    tile_tmfma(acc, A, R);
    if (wave == 4) STAMP(36);
    if (tile64_partial(acc, lds + L_P1 + u * 512, lds + L_FP, lane)) {
      STAMP(37);
      const Tag tag = mk_tag(epoch_hash(ep));
#pragma unroll
      for (int i = 0; i < 4; ++i) {
        const f32x2 s = tile64_sum(lds + L_P1, i, lane);
        st16_sc1(piece(s[0], s[1], tag), ws,
                 WS_Q + (gh * 32 + ((tix & 63) >> 1)) * 2048 + (tix >> 6) * 1024 + (tix & 1) * 512 + (lane & 31) * 16, i * 2 * 32 * 2048);
      }
      FENCE();
      if (lane == 0) __hip_atomic_store(lds + L_FP, 5u, RLX_WG);
      STAMP(9);
    }
  } else if (t_ffn) {
    u32x4 R[32];
#pragma unroll
    for (int i = 0; i < 8; ++i) R[i] = R0[i];
#pragma unroll
    for (int i = 8; i < 32; ++i) { vm_wait<WIN_F - 1>(); R[i] = ld16<W_AUX>(rw1, voff1, ((i >> 3) * 64 + (i & 7)) * ld2); FENCE(); }
    if (wave == 4) STAMP(34);
    NEXT_FIRST();
    lds_wait(lds + L_FX, (unsigned)NS);
    u32x4 A[4]; f32x4 acc[8];
#pragma unroll
    for (int ks = 0; ks < 4; ++ks) A[ks] = *(LAS u32x4*)(lds + L_XN + c7 * XN_STRIDE + (k0 >> 1) + ks * 32 + ch * 16 + g * 4);
    tile_tmfma(acc, A, R);
    if (tile64_partial(acc, lds + L_P1 + u * 512, lds + L_FP, lane)) {
      const Tag tag = mk_tag(epoch_hash(ep));
#pragma unroll
      for (int i = 0; i < 2; ++i) {
        f32x4 s = {0.f, 0.f, 0.f, 0.f};
#pragma unroll
        for (int w = 0; w < 4; ++w) s += *(LAS f32x4*)(lds + L_P1 + w * 512 + i * 256 + lane * 4);
        st16_sc1(piece(pk_bf16(s[0], s[1]), pk_bf16(s[2], s[3]), tag), ws,
                 (tix < NFT ? WS_G + tix * 256 : WS_U + (tix - NFT) * 256) + (g * F + c * 4) * 4, i * 4 * F * 4);
      }
      FENCE();
      if (lane == 0) __hip_atomic_store(lds + L_FP, 5u, RLX_WG);
      STAMP(10);
    }
  } else {
    NEXT_FIRST();
  }
  if (wave == 7) {
    const u64 z7 = epoch_hash(ep);
    const int col = b * 128 + (h & 1) * 64 + c7 * 8 + ((h >> 1) & 1) * 4;
    const u32x2 xres = *(const GAS u32x2*)(a.x + (h >> 2) * D + col);
    if (ADAPT) {
      const Tag tg = mk_tag(z7);
      lds_wait<LSLEEP>(lds + L_FM, 1u);
      if (lane == 0) st16_sc1(piece(lds[L_M], lds[L_M + 1], tg), ws, WS_M + wg * 16, 0);
      if (wg == M_WG) {
        u32x4 v[4];
        gather<16>(v, ws, WS_M + lane * 16, true, tg, [](int i) { return i * 1024; });
        const unsigned ref = lds[L_M + 1];
        float sa = 0.f, sb = 0.f;
#pragma unroll
        for (int i = 0; i < 4; ++i) { sa += (float)(int)(v[i][0] - ref); sb += (float)(int)(v[i][2] - ref); }
        sa = xrow_sum(row16_sum(sa)) * (1.f / 256); sb = xrow_sum(row16_sum(sb)) * (1.f / 256);
        const float tau = (sb - sa) * (M_K / (M_B - M_A));
        if (lane < 32) st16_sc1(piece(ref + (unsigned)(int)(sb - M_B * tau) - PACE_B - M_OFF, (unsigned)(tau * 16.f + 0.5f), tg), ws, WS_C + lane * 128, 0);
      }
      u32x4 v[1];
      gather_init(v, true, tg);
      for (int n = 0; n < 24; ++n) {
        __builtin_amdgcn_s_sleep(24);
        if (gather_pass(v, ws, WS_C + (blockIdx.x >> 3) * 128, tg, [](int) { return 0; })) {
          if (lane == 0) *(volatile LAS u32x2*)(lds + L_CT) = u32x2{v[0][0], v[0][2]};
          STAMP(38);
          break;
        }
      }
    }
    lds_wait<LSLEEP>(lds + L_FE, 1u);
    __builtin_amdgcn_s_setprio(2);
    lds_wait(lds + L_FO, 1u);
    if (D_P) __builtin_amdgcn_s_sleep(D_P);
    u32x4 p[4];
    gather<S_P>(p, ws, WS_P + (b * 32 + h) * 4096 + lane * 16, true, mk_tag12(z7), [](int i) { return i * 1024; });
    float y[4];
#pragma unroll
    for (int e = 0; e < 4; ++e) { y[e] = (dec20(p[0][e]) + dec20(p[1][e])) + (dec20(p[2][e]) + dec20(p[3][e])); y[e] += dpp<XOR8>(y[e]); y[e] = xrow_sum(y[e]); }
    const u32x2 res = {pk_bf16(bf_lo(xres[0]) + y[0], bf_hi(xres[0]) + y[1]), pk_bf16(bf_lo(xres[1]) + y[2], bf_hi(xres[1]) + y[3])};
    if (lane < 8) __builtin_amdgcn_raw_buffer_store_b64(res, mk_rsrc(a.out, B * D * 2), ((h >> 2) * D + col) * 2, 0, OUT_SC1 ? AUX_SC1 : 0);
    if (wg == 0 && lane == 0) st8_sc1(u32x2{(unsigned)z7, (unsigned)(z7 >> 32)}, ws, WS_EPOCH, 0);
    STAMP(18);
    STAMP_FLUSH();
    return;
  }
  if (wave == 6 && !t_q) { STAMP_FLUSH(); return; }

  u32x4 W[16];
  f32x4 acc[4];
  u64 z;
  if (wave >= 5) {
    f32x4 a8[8];
    z = 0;
    if (t_q) {
      LAS unsigned* ai = lds + L_AI + (wave - 5) * 8 * AI_STRIDE;
      u32x4 R[32];
      tile_load<32, WIN_S>(R, N0, rw2, voff, g >= 2 ? 0x7ffffff0 : voff, 32 * D * 2, D * 2);
      if (wave == 5) STAMP(19);
      const Tag tag = mk_tag(epoch_hash(ep));
      u32x4 v[8];
      gather(v, ws, WS_G + (gh * F + f0 + (lane & 31) * 4) * 4, (lane & 31) < FS / 4, tag, [](int i) { return i * 2 * F * 4; });
      if (wave == 5) STAMP(21);
#pragma unroll
      for (int i = 0; i < 4; ++i) {
        const u32x4 gt = v[i], up = v[4 + i];
        *(LAS u32x2*)(ai + (2 * i + gh) * AI_STRIDE + (lane & 31) * 2) =
            u32x2{pk_bf16(bf_lo(gt[0]) * bf_lo(up[0]), bf_hi(gt[0]) * bf_hi(up[0])), pk_bf16(bf_lo(gt[2]) * bf_lo(up[2]), bf_hi(gt[2]) * bf_hi(up[2]))};
      }
      LDS_DONE();
      u32x4 A[4];
#pragma unroll
      for (int ks = 0; ks < 4; ++ks) A[ks] = *(LAS u32x4*)(ai + c7 * AI_STRIDE + ks * 16 + g * 4);
      tile_tmfma(a8, A, R);
      if (wave == 6) {
#pragma unroll
        for (int e = 0; e < 8; ++e) *(LAS f32x4*)(lds + L_A6 + e * 256 + lane * 4) = a8[e];
        LDS_DONE();
        if (lane == 0) __hip_atomic_store(lds + L_F6, 1u, RLX_WG);
        STAMP_FLUSH();
        return;
      }
      lds_wait(lds + L_F6, 1u);
#pragma unroll
      for (int e = 0; e < 8; ++e) a8[e] += *(LAS f32x4*)(lds + L_A6 + e * 256 + lane * 4);
    } else {
#pragma unroll
      for (int e = 0; e < 8; ++e) a8[e] = f32x4{0.f, 0.f, 0.f, 0.f};
    }
    z = epoch_hash(ep);
#pragma unroll
    for (int e = 0; e < 4; ++e) { *(LAS f32x4*)(lds + L_ACC + e * 256 + lane * 4) = a8[e]; acc[e] = a8[4 + e]; }
    LDS_DONE();
    if (lane == 0) __hip_atomic_store(lds + L_FA, 1u, RLX_WG);
    lds_wait(lds + L_FW, 1u);
#pragma unroll
    for (int i = 0; i < 16; ++i) { W[i] = *(LAS u32x4*)(lds + L_WO + i * 256 + lane * 4); asm volatile("" : "+v"(W[i])); }
    STAMP(22);
  } else {
    {
      u32x4 R[32];
      tile_load<32, WIN_S>(R, N0, rw2, voff, voff, 32 * D * 2, D * 2);
#pragma unroll
      for (int ks = 0; ks < 4; ++ks)
#pragma unroll
        for (int e = 0; e < 4; ++e) {
          W[ks * 4 + e] = tile_frag(R, ks, e);
          asm volatile("" : "+v"(W[ks * 4 + e]));
          *(LAS u32x4*)(lds + L_WO + (ks * 4 + e) * 256 + lane * 4) = tile_frag(R, ks, 4 + e);
        }
      LDS_DONE();
      if (lane == 0) __hip_atomic_store(lds + L_FW, 1u, RLX_WG);
    }
    z = epoch_hash(ep);
    STAMP(13);
    lds_wait(lds + L_FA, 1u);
#pragma unroll
    for (int e = 0; e < 4; ++e) { acc[e] = *(LAS f32x4*)(lds + L_ACC + e * 256 + lane * 4); asm volatile("" : "+v"(acc[e])); }
  }
  lds_wait<LSLEEP>(lds + L_FE, 1u);
  __builtin_amdgcn_s_setprio(2);
  lds_wait(lds + L_FS, (unsigned)NS);
  LAS unsigned* ai = lds + L_AI + u * 8 * AI_STRIDE;
  if (D_O) __builtin_amdgcn_s_sleep(D_O);
  {
    u32x4 v[4];
    gather<S_O>(v, ws, WS_O + h * 4096 + lane * 16, true, mk_tag(z), [](int i) { return i * 1024; });
    if (wave == 4) { if (lane == 0) __hip_atomic_store(lds + L_FO, 1u, RLX_WG); STAMP(16); }
#pragma unroll
    for (int i = 0; i < 4; ++i) *(LAS u32x2*)(ai + (2 * i + gh) * AI_STRIDE + (lane & 31) * 2) = u32x2{v[i][0], v[i][2]};
    LDS_DONE();
  }
#pragma unroll
  for (int ks = 0; ks < 4; ++ks) {
    const u32x4 A = *(LAS u32x4*)(ai + c7 * AI_STRIDE + ks * 16 + g * 4);
#pragma unroll
    for (int e = 0; e < 4; ++e) acc[e] = mfma(A, W[ks * 4 + e], acc[e]);
  }
  const Tag12 tag = mk_tag12(z);
#pragma unroll
  for (int r = 0; r < 2; ++r) {
    u32x4 o;
#pragma unroll
    for (int e = 0; e < 4; ++e) o[e] = enc20(gh ? acc[e][2 + r] : acc[e][r], tag.t[e]);
    st16_sc1(o, ws, WS_P + ((b * 32 + (4 * g1 + 2 * gh) * 4 + u * 2 + ch) * 32 + h) * 128 + c7 * 16, r * 4 * 32 * 128);
  }
  if (wave == 4) STAMP(17);
  STAMP_FLUSH();
  asm volatile(".Lfd_mark_b:");
}

}

extern "C" void kernel_launch(void* const* d_in, const int* in_sizes, int n_in, void* d_out, int out_size, void* d_ws, size_t ws_size,
                              hipStream_t stream) {
  Args a;
  a.x = (const unsigned short*)d_in[0];
  a.kc = (const unsigned short*)d_in[1];
  a.vc = (const unsigned short*)d_in[2];
  a.wq = (const unsigned short*)d_in[3];
  a.wo = (const unsigned short*)d_in[4];
  a.wg = (const unsigned short*)d_in[6];
  a.wu = (const unsigned short*)d_in[7];
  a.wd = (const unsigned short*)d_in[8];
  a.out = (unsigned short*)d_out;
  a.ws = (unsigned char*)d_ws;
  fused_decode<<<B * H, 512, 0, stream>>>(a);
}
```
